```python
import torch

def _softmax(x, axis=-1):
    return torch.softmax(x, dim=axis)
def _gelu(x):
    return torch.nn.functional.gelu(x, approximate="tanh")
def _silu(x):
    return torch.nn.functional.silu(x)
def _sigmoid(x):
    return torch.sigmoid(x)
def _layernorm(x, axis=-1, eps=1e-5):
    m = x.mean(dim=axis, keepdim=True)
    v = x.var(dim=axis, keepdim=True, unbiased=False)
    return (x - m) / torch.sqrt(v + eps)
def _scatter_add(tgt, idx, src, axis=0):
    return tgt.clone().index_add_(axis, idx, src)
def _segment_sum(src, seg, n):
    out = torch.zeros((n,) + src.shape[1:], dtype=src.dtype)
    return out.index_add_(0, seg, src)
def _pad(x, pw):
    flat = tuple(v for pair in reversed(pw) for v in pair)
    return torch.nn.functional.pad(x, flat)

kernel_name = 'gated_mix__mha_block_cached__sliding_block_cached__decode'

def setup_inputs():

    _w = (lambda a: (lambda t: t.to(torch.bfloat16) if t.is_floating_point() else t)(torch.as_tensor(a)))
    def _A_setup():
        B, Sq, D = 4, 1, 512
        H, Sk, Dh = 32, 4096, 128
        return {
            "x": _w((lambda *s: torch.randn(*s, dtype=torch.bfloat16) * 0.05)(B, Sq, D)),
            "k_cache": _w((lambda *s: torch.randn(*s, dtype=torch.bfloat16) * 6.4)(B, H, Sk, Dh)),
            "v_cache": _w((lambda *s: torch.randn(*s, dtype=torch.bfloat16) * 0.05)(B, H, Sk, Dh)),
            "wq": _w((lambda *s: torch.randn(*s, dtype=torch.bfloat16) * 6.4)(D, H * Dh) * (D ** -0.5)),
            "wo": _w((lambda *s: torch.randn(*s, dtype=torch.bfloat16) * 0.05)(H * Dh, D) * ((H * Dh) ** -0.5)),
        }
    _A_in = _A_setup()

    def _B_setup():
        B, Sq, D = 4, 1, 512
        Hq, Hkv, Sk, Dh = 32, 32, 4096, 128
        assert Hq % Hkv == 0, (Hq, Hkv)
        return {
            "x": _w((lambda *s: torch.randn(*s, dtype=torch.bfloat16) * 0.05)(B, Sq, D)),
            "k_cache": _w((lambda *s: torch.randn(*s, dtype=torch.bfloat16) * 6.4)(B, Hkv, Sk, Dh)),
            "v_cache": _w((lambda *s: torch.randn(*s, dtype=torch.bfloat16) * 0.05)(B, Hkv, Sk, Dh)),
            "wq": _w((lambda *s: torch.randn(*s, dtype=torch.bfloat16) * 6.4)(D, Hq * Dh) * (D ** -0.5)),
            "wo": _w((lambda *s: torch.randn(*s, dtype=torch.bfloat16) * 0.05)(Hq * Dh, D) * ((Hq * Dh) ** -0.5)),
        }
    _B_in = _B_setup()

    return {
        "A_x": _A_in["x"],
        "A_k_cache": _A_in["k_cache"],
        "A_v_cache": _A_in["v_cache"],
        "A_wq": _A_in["wq"],
        "A_wo": _A_in["wo"],
        "B_x": _B_in["x"],
        "B_k_cache": _B_in["k_cache"],
        "B_v_cache": _B_in["v_cache"],
        "B_wq": _B_in["wq"],
        "B_wo": _B_in["wo"],
        "gate_w": _w((lambda *s: torch.randn(*s, dtype=torch.bfloat16) * 0.1)(512) * 16.0),
    }

def reference(A_x, A_k_cache, A_v_cache, A_wq, A_wo, B_x, B_k_cache, B_v_cache, B_wq, B_wo, gate_w):
    def _A_ref(x, k_cache, v_cache, wq, wo):
        B, Sq, D = x.shape
        H, Dh = 32, 128
        q = (lambda x, *d: x.permute(*d))((x @ wq).reshape(B, Sq, H, Dh), 0, 2, 1, 3)
        scores = torch.einsum("bhqd,bhkd->bhqk", q, k_cache) * (Dh ** -0.5)
        p = _softmax(scores, axis=-1)
        o = torch.einsum("bhqk,bhkd->bhqd", p, v_cache)
        return (lambda x, *d: x.permute(*d))(o, 0, 2, 1, 3).reshape(B, Sq, H * Dh) @ wo

    def _B_ref(x, k_cache, v_cache, wq, wo):
        B, Sq, D = x.shape
        Hq, Dh = 32, 128
        Hkv, Sk = k_cache.shape[1], k_cache.shape[2]
        W = min(4096, max(256, Sk // 4))
        rep = Hq // Hkv
        q = (lambda x, *d: x.permute(*d))((x @ wq).reshape(B, Sq, Hq, Dh), 0, 2, 1, 3)
        k = (lambda x, n, axis: torch.repeat_interleave(x, n, dim=axis))(k_cache[:, :, Sk - W:, :], rep, axis=1)
        v = (lambda x, n, axis: torch.repeat_interleave(x, n, dim=axis))(v_cache[:, :, Sk - W:, :], rep, axis=1)
        scores = torch.einsum("bhqd,bhkd->bhqk", q, k) * (Dh ** -0.5)
        p = _softmax(scores, axis=-1)
        o = torch.einsum("bhqk,bhkd->bhqd", p, v)
        return (lambda x, *d: x.permute(*d))(o, 0, 2, 1, 3).reshape(B, Sq, Hq * Dh) @ wo

    x = A_x
    g = _sigmoid((x * gate_w).sum(-1, keepdims=True))
    a = _A_ref(x, A_k_cache, A_v_cache, A_wq, A_wo)
    b = _B_ref(x, B_k_cache, B_v_cache, B_wq, B_wo)
    return g * a + (1 - g) * b
```

```cpp
#include <hip/hip_runtime.h>
#include <hip/hip_bf16.h>
#include <stdint.h>

typedef unsigned u32x4 __attribute__((ext_vector_type(4)));
typedef float f32x4 __attribute__((ext_vector_type(4)));
typedef float f32x2 __attribute__((ext_vector_type(2)));
typedef short bf16x8 __attribute__((ext_vector_type(8)));
typedef __bf16 bf16x2_t __attribute__((ext_vector_type(2)));
typedef unsigned long long u64;

namespace {

constexpr int NB = 4, DM = 512, NH = 32, SK = 4096, DH = 128, WINB = 1024;
constexpr int HD = NH * DH;
constexpr int UA = SK / 16, UB = WINB / 16;
constexpr int UH = UA + UB;
#define K1_UE 40
constexpr int UE = K1_UE, UO = 80 - UE;
static_assert(UE > 0 && UO > 0 && UE <= 60 && UO <= 60, "a workgroup spans at most two segments");
constexpr int NWG = 256, NTHR = 1024;
constexpr int NSLOT = NWG + 64;
constexpr float QSCALE = 0.088388347648318440f * 1.4426950408889634f;

constexpr int L_QP = 0;
constexpr int L_RED = 8192;
constexpr int L_REC = L_RED + 16384;
constexpr int L_QS = L_REC + 2 * 16 * 132;
constexpr int L_TOT = L_QS + 512;

typedef const __attribute__((address_space(1))) u32x4* gptr4;
typedef const __attribute__((address_space(1))) char* gchar;
__device__ __forceinline__ u32x4 ld_nt(u64 sbase, unsigned voff) { return __builtin_nontemporal_load((gptr4)((gchar)sbase + voff)); }
__device__ __forceinline__ u32x4 ld_df(const char* p) { return *(const u32x4*)p; }
__device__ __forceinline__ float dot2(unsigned k, unsigned q, float acc) {
  return __builtin_amdgcn_fdot2_f32_bf16(__builtin_bit_cast(bf16x2_t, k), __builtin_bit_cast(bf16x2_t, q), acc, false);
}
template <int CTRL> __device__ __forceinline__ float dpp(float x) {
  return __builtin_bit_cast(float, __builtin_amdgcn_mov_dpp(__builtin_bit_cast(int, x), CTRL, 0xf, 0xf, true));
}
__device__ __forceinline__ float row16_sum(float x) {
  x += dpp<0xB1>(x);
  x += dpp<0x4E>(x);
  x += dpp<0x141>(x);
  x += dpp<0x140>(x);
  return x;
}
__device__ __forceinline__ float xrow_max(float x) {
  auto s = __builtin_amdgcn_permlane16_swap(__float_as_uint(x), __float_as_uint(x), false, false);
  x = fmaxf(__uint_as_float(s[0]), __uint_as_float(s[1]));
  auto t = __builtin_amdgcn_permlane32_swap(__float_as_uint(x), __float_as_uint(x), false, false);
  return fmaxf(__uint_as_float(t[0]), __uint_as_float(t[1]));
}
__device__ __forceinline__ float xrow_sum(float x) {
  auto s = __builtin_amdgcn_permlane16_swap(__float_as_uint(x), __float_as_uint(x), false, false);
  x = __uint_as_float(s[0]) + __uint_as_float(s[1]);
  auto t = __builtin_amdgcn_permlane32_swap(__float_as_uint(x), __float_as_uint(x), false, false);
  return __uint_as_float(t[0]) + __uint_as_float(t[1]);
}
__device__ __forceinline__ unsigned short f2bf(float f) { return __builtin_bit_cast(unsigned short, (__bf16)f); }
__device__ __forceinline__ float bf2f(unsigned short h) { return __uint_as_float((unsigned)h << 16); }
__device__ __forceinline__ unsigned pack_bf(float lo, float hi) { return (unsigned)f2bf(lo) | ((unsigned)f2bf(hi) << 16); }

struct Split { int seg0, n0, n1, lu0; };
__host__ __device__ __forceinline__ Split split_of(int bid) {
  const int xcd = bid & 7, idx = bid >> 3, pair = xcd >> 1, odd = xcd & 1;
  const int u0 = odd ? 32 * UE + idx * UO : idx * UE, n = odd ? UO : UE;
  const int hh = u0 / UH, rem = u0 - hh * UH, br = rem >= UA ? 1 : 0;
  const int end0 = br ? (hh + 1) * UH : hh * UH + UA;
  Split s;
  s.seg0 = (pair * 8 + hh) * 2 + br;
  s.n0 = (u0 + n < end0 ? u0 + n : end0) - u0;
  s.n1 = n - s.n0;
  s.lu0 = br ? rem - UA : rem;
  return s;
}

__device__ __forceinline__ bf16x8 col_frag(const u32x4 (&W)[8], int e) {
  const unsigned sel = (e & 1) ? 0x07060302u : 0x05040100u;
  u32x4 f;
#pragma unroll
  for (int q = 0; q < 4; ++q) f[q] = __builtin_amdgcn_perm(W[2 * q + 1][e >> 1], W[2 * q][e >> 1], sel);
  return __builtin_bit_cast(bf16x8, f);
}

struct MainArgs {
  const char* x;
  const char* kA; const char* vA; const char* wqA; const char* woA;
  const char* kB; const char* vB; const char* wqB; const char* woB;
  float* y;
  float* ml;
};

__global__ void __launch_bounds__(NTHR) k_main(MainArgs a) {
  __shared__ __attribute__((aligned(16))) float lds[L_TOT];
  const int tid = threadIdx.x, lane = tid & 63;
  const int wave = __builtin_amdgcn_readfirstlane(tid >> 6);
  const int r = lane >> 4, c = lane & 15;
  const int b = wave >> 2, sub = wave & 3;
  const Split sp = split_of(blockIdx.x);
  const int seg0 = sp.seg0, seg1 = sp.seg0 + 1, n0 = sp.n0, n1 = sp.n1;
  const bool mixed = n1 > 0;
  const int h0 = seg0 >> 1, br0 = seg0 & 1, h1 = (seg1 >> 1) & (NH - 1), br1 = seg1 & 1;
  const int g0 = (n0 + 3) >> 2, g1 = (n1 + 3) >> 2;
  const int NG = (g0 + g1 + 1) & ~1;
  const u64 off0 = ((u64)((b * NH + h0) * SK + (br0 ? SK - WINB : 0) + sp.lu0 * 16 + sub * 4)) * 256;
  const u64 off1 = ((u64)((b * NH + h1) * SK + (br1 ? SK - WINB : 0) + sub * 4)) * 256;
  const u64 k0 = (u64)(br0 ? a.kB : a.kA) + off0, v0 = (u64)(br0 ? a.vB : a.vA) + off0;
  const u64 k1 = (u64)(br1 ? a.kB : a.kA) + off1, v1 = (u64)(br1 ? a.vB : a.vA) + off1;
  const unsigned lane16 = lane * 16;

  u32x4 A[8], Bf[8];

#define ISSUE(GI, BUF)                                                                       \
  {                                                                                          \
    const int gi_ = (GI);                                                                    \
    const bool s1_ = gi_ >= g0 && mixed;                                                     \
    const int jb_ = (s1_ ? gi_ - g0 : gi_) * 4, nn_ = s1_ ? n1 : n0;                         \
    const u64 kb_ = s1_ ? k1 : k0, vb_ = s1_ ? v1 : v0;                                      \
    _Pragma("unroll") for (int i_ = 0; i_ < 4; ++i_) {                                       \
      const int j_ = jb_ + i_;                                                               \
      const bool ok_ = j_ < nn_;                                                             \
      const u64 so_ = (u64)(ok_ ? j_ : 0) * 4096;                                            \
      const unsigned vo_ = ok_ ? lane16 : 0u;                                                \
      BUF[i_] = ld_nt(kb_ + so_, vo_);                                                       \
      BUF[4 + i_] = ld_nt(vb_ + so_, vo_);                                                   \
    }                                                                                        \
    __builtin_amdgcn_sched_barrier(0);                                                       \
  }

  const int m16 = lane & 15, g4 = lane >> 4;
  u32x4 xa = {0u, 0u, 0u, 0u};
  if (m16 < NB) xa = ld_df(a.x + (m16 * DM + wave * 32 + g4 * 8) * 2);
  {
    const char* wq = (br0 ? a.wqB : a.wqA) + ((size_t)(wave * 32 + g4 * 8) * HD + h0 * DH + c * 8) * 2;
#pragma unroll
    for (int j = 0; j < 8; ++j) Bf[j] = ld_df(wq + (size_t)j * HD * 2);
  }
  __builtin_amdgcn_sched_barrier(0);
  ISSUE(0, A)
  unsigned short* qs = (unsigned short*)(lds + L_QS);
#define QPROJ(SEGI)                                                                          \
  {                                                                                          \
    f32x4 d_[8];                                                                             \
    _Pragma("unroll") for (int e = 0; e < 8; ++e)                                            \
      d_[e] = __builtin_amdgcn_mfma_f32_16x16x32_bf16(__builtin_bit_cast(bf16x8, xa), col_frag(Bf, e), f32x4{0.f, 0.f, 0.f, 0.f}, 0, 0, 0); \
    if (lane < 16) {                                                                         \
      _Pragma("unroll") for (int bb = 0; bb < 4; ++bb) {                                     \
        float* dst = lds + L_QP + (wave * 4 + bb) * 128 + c * 8;                             \
        *(f32x4*)dst = f32x4{d_[0][bb], d_[1][bb], d_[2][bb], d_[3][bb]};                    \
        *(f32x4*)(dst + 4) = f32x4{d_[4][bb], d_[5][bb], d_[6][bb], d_[7][bb]};              \
      }                                                                                      \
    }                                                                                        \
  }
#define QFIN(SEGI)                                                                           \
  {                                                                                          \
    __syncthreads();                                                                         \
    if (tid < 512) {                                                                         \
      float s_ = 0.f;                                                                        \
      _Pragma("unroll") for (int w = 0; w < 16; ++w) s_ += lds[L_QP + w * 512 + tid];        \
      qs[(SEGI) * 512 + tid] = f2bf(s_);                                                     \
    }                                                                                        \
    __syncthreads();                                                                         \
  }
  QPROJ(0)
  if (mixed) {
    const char* wq = (br1 ? a.wqB : a.wqA) + ((size_t)(wave * 32 + g4 * 8) * HD + h1 * DH + c * 8) * 2;
#pragma unroll
    for (int j = 0; j < 8; ++j) Bf[j] = ld_df(wq + (size_t)j * HD * 2);
  }
  QFIN(0)
  if (mixed) {
    QPROJ(1)
    QFIN(1)
  }
  u32x4 q = *(const u32x4*)(qs + b * 128 + c * 8);

  float m = -INFINITY, l = 0.f, acc[8];
#pragma unroll
  for (int k = 0; k < 8; ++k) acc[k] = 0.f;

#define FLUSH(SEGI)                                                                          \
  {                                                                                          \
    const float M_ = xrow_max(m);                                                            \
    const float sc_ = (m == -INFINITY) ? 0.f : __builtin_amdgcn_exp2f(m - M_);               \
    const float L_ = xrow_sum(l * sc_);                                                      \
    float o_[8];                                                                             \
    _Pragma("unroll") for (int k = 0; k < 8; ++k) o_[k] = xrow_sum(acc[k] * sc_);            \
    float* rec_ = lds + L_REC + ((SEGI) * 16 + wave) * 132;                                  \
    if (lane < 16) {                                                                         \
      *(f32x4*)(rec_ + 4 + c * 8) = f32x4{o_[0], o_[1], o_[2], o_[3]};                       \
      *(f32x4*)(rec_ + 8 + c * 8) = f32x4{o_[4], o_[5], o_[6], o_[7]};                       \
      if (lane == 0) { rec_[0] = M_; rec_[1] = L_; }                                         \
    }                                                                                        \
  }

#define COMPUTE(GI, BUF)                                                                     \
  {                                                                                          \
    const int gi_ = (GI);                                                                    \
    if (mixed && gi_ == g0) {                                                                \
      FLUSH(0)                                                                               \
      m = -INFINITY; l = 0.f;                                                                \
      _Pragma("unroll") for (int k = 0; k < 8; ++k) acc[k] = 0.f;                            \
      q = *(const u32x4*)(qs + 512 + b * 128 + c * 8);                                       \
    }                                                                                        \
    const bool s1_ = gi_ >= g0 && mixed;                                                     \
    const int jb_ = (s1_ ? gi_ - g0 : gi_) * 4, nn_ = s1_ ? n1 : n0;                         \
    float t_[4];                                                                             \
    _Pragma("unroll") for (int i_ = 0; i_ < 4; ++i_) {                                       \
      float d_ = dot2(BUF[i_][0], q[0], 0.f);                                                \
      d_ = dot2(BUF[i_][1], q[1], d_);                                                       \
      d_ = dot2(BUF[i_][2], q[2], d_);                                                       \
      d_ = dot2(BUF[i_][3], q[3], d_);                                                       \
      d_ = row16_sum(d_) * QSCALE;                                                           \
      t_[i_] = (jb_ + i_ < nn_) ? d_ : -INFINITY;                                            \
    }                                                                                        \
    const float mn_ = fmaxf(fmaxf(m, fmaxf(t_[0], t_[1])), fmaxf(t_[2], t_[3]));             \
    const float ms_ = (mn_ == -INFINITY) ? 0.f : mn_;                                        \
    const float al_ = __builtin_amdgcn_exp2f(m - ms_);                                       \
    m = mn_;                                                                                 \
    l *= al_;                                                                                \
    _Pragma("unroll") for (int k = 0; k < 8; ++k) acc[k] *= al_;                             \
    _Pragma("unroll") for (int i_ = 0; i_ < 4; ++i_) {                                       \
      const float p_ = __builtin_amdgcn_exp2f(t_[i_] - ms_);                                 \
      l += p_;                                                                               \
      _Pragma("unroll") for (int k = 0; k < 4; ++k) {                                        \
        const unsigned w_ = BUF[4 + i_][k];                                                  \
        acc[2 * k] = fmaf(p_, __uint_as_float(w_ << 16), acc[2 * k]);                        \
        acc[2 * k + 1] = fmaf(p_, __uint_as_float(w_ & 0xffff0000u), acc[2 * k + 1]);        \
      }                                                                                      \
    }                                                                                        \
    __builtin_amdgcn_sched_barrier(0);                                                       \
  }

  int gi = 0;
#pragma unroll 1
  for (; gi + 2 < NG; gi += 2) {
    ISSUE(gi + 1, Bf)
    COMPUTE(gi, A)
    ISSUE(gi + 2, A)
    COMPUTE(gi + 1, Bf)
  }
  ISSUE(gi + 1, Bf)
  COMPUTE(gi, A)
  const int rb = wave >> 2, cb = wave & 3;
  const size_t wo_off = ((size_t)(rb * 32 + g4 * 8) * DM + cb * 128 + c * 8) * 2;
  {
    const char* wo = (br0 ? a.woB : a.woA) + (size_t)h0 * DH * DM * 2 + wo_off;
#pragma unroll
    for (int j = 0; j < 8; ++j) A[j] = ld_df(wo + j * DM * 2);
    __builtin_amdgcn_sched_barrier(0);
  }
  COMPUTE(gi + 1, Bf)
  if (mixed) {
    const char* wo = (br1 ? a.woB : a.woA) + (size_t)h1 * DH * DM * 2 + wo_off;
#pragma unroll
    for (int j = 0; j < 8; ++j) Bf[j] = ld_df(wo + j * DM * 2);
    FLUSH(1)
  } else {
    FLUSH(0)
  }
  __syncthreads();

#define OPROJ(SEGI, BUF, SLOT)                                                               \
  {                                                                                          \
    u32x4 ahi = {0u, 0u, 0u, 0u}, alo = {0u, 0u, 0u, 0u};                                    \
    {                                                                                        \
      const int bb = m16 & 3;                                                                \
      const float* rec_ = lds + L_REC + ((SEGI) * 16 + bb * 4) * 132;                        \
      float mw_[4], lw_[4];                                                                  \
      _Pragma("unroll") for (int w = 0; w < 4; ++w) { mw_[w] = rec_[w * 132]; lw_[w] = rec_[w * 132 + 1]; } \
      const float M_ = fmaxf(fmaxf(mw_[0], mw_[1]), fmaxf(mw_[2], mw_[3]));                  \
      float o_[8], L_ = 0.f;                                                                 \
      _Pragma("unroll") for (int k = 0; k < 8; ++k) o_[k] = 0.f;                             \
      _Pragma("unroll") for (int w = 0; w < 4; ++w) {                                        \
        const float sw_ = (mw_[w] == -INFINITY) ? 0.f : __builtin_amdgcn_exp2f(mw_[w] - M_); \
        L_ = fmaf(sw_, lw_[w], L_);                                                          \
        const f32x4 u0_ = *(const f32x4*)(rec_ + w * 132 + 4 + rb * 32 + g4 * 8);            \
        const f32x4 u1_ = *(const f32x4*)(rec_ + w * 132 + 8 + rb * 32 + g4 * 8);            \
        _Pragma("unroll") for (int k = 0; k < 4; ++k) { o_[k] = fmaf(sw_, u0_[k], o_[k]); o_[4 + k] = fmaf(sw_, u1_[k], o_[4 + k]); } \
      }                                                                                      \
      if (m16 < NB) {                                                                        \
        _Pragma("unroll") for (int k = 0; k < 4; ++k) {                                      \
          const unsigned short h0_ = f2bf(o_[2 * k]), h1_ = f2bf(o_[2 * k + 1]);             \
          ahi[k] = (unsigned)h0_ | ((unsigned)h1_ << 16);                                    \
          alo[k] = pack_bf(o_[2 * k] - bf2f(h0_), o_[2 * k + 1] - bf2f(h1_));                \
        }                                                                                    \
      }                                                                                      \
      if (wave == 0 && lane < NB) *(f32x2*)(a.ml + ((size_t)(SLOT) * NB + lane) * 2) = f32x2{M_, L_}; \
    }                                                                                        \
    f32x4 d_[8];                                                                             \
    _Pragma("unroll") for (int e = 0; e < 8; ++e) {                                          \
      const bf16x8 f_ = col_frag(BUF, e);                                                    \
      d_[e] = __builtin_amdgcn_mfma_f32_16x16x32_bf16(__builtin_bit_cast(bf16x8, ahi), f_, f32x4{0.f, 0.f, 0.f, 0.f}, 0, 0, 0); \
      d_[e] = __builtin_amdgcn_mfma_f32_16x16x32_bf16(__builtin_bit_cast(bf16x8, alo), f_, d_[e], 0, 0, 0); \
    }                                                                                        \
    if (lane < 16) {                                                                         \
      _Pragma("unroll") for (int bb = 0; bb < 4; ++bb) {                                     \
        float* dst = lds + L_RED + (SEGI) * 8192 + (rb * 4 + bb) * 512 + cb * 128 + c * 8;   \
        *(f32x4*)dst = f32x4{d_[0][bb], d_[1][bb], d_[2][bb], d_[3][bb]};                    \
        *(f32x4*)(dst + 4) = f32x4{d_[4][bb], d_[5][bb], d_[6][bb], d_[7][bb]};              \
      }                                                                                      \
    }                                                                                        \
  }
  OPROJ(0, A, blockIdx.x)
  if (mixed) OPROJ(1, Bf, NWG + seg1)
  __syncthreads();
  {
    const int sg = tid >> 9, t = tid & 511;
    if (sg == 0 || mixed) {
      const float* src = lds + L_RED + sg * 8192 + t * 4;
      f32x4 s = *(const f32x4*)src;
#pragma unroll
      for (int k = 1; k < 4; ++k) s += *(const f32x4*)(src + k * 2048);
      const int slot = sg == 0 ? (int)blockIdx.x : NWG + seg1;
      *(f32x4*)(a.y + (size_t)slot * 2048 + t * 4) = s;
    }
  }
}

constexpr int K2_THR = 512;
struct FinArgs {
  const float* y; const float* ml; const unsigned short* x; const unsigned short* gw; unsigned short* out;
};
__device__ __forceinline__ bool sec_valid(int seg) {
  const int ls = seg & 15, start = (ls >> 1) * UH + (ls & 1) * UA;
  return start < 32 * UE ? (start % UE) != 0 : ((start - 32 * UE) % UO) != 0;
}
__global__ void __launch_bounds__(K2_THR) k_final(FinArgs a) {
  __shared__ float s_m[NSLOT], s_l[NSLOT], s_w[NSLOT], s_M[64], s_iL[64], s_g[8];
  __shared__ __attribute__((aligned(16))) float s_red[64 * 32];
  const int tid = threadIdx.x, b = blockIdx.x >> 4, cg = blockIdx.x & 15;
  const int ch = tid & 7, sg = tid >> 3;
  f32x4 yv[5];
#pragma unroll
  for (int k = 0; k < 5; ++k) {
    const int slot = sg + 64 * k;
    const bool ok = k < 4 || sec_valid(sg);
    yv[k] = ok ? *(const f32x4*)(a.y + (size_t)slot * 2048 + b * 512 + cg * 32 + ch * 4) : f32x4{0.f, 0.f, 0.f, 0.f};
  }
  int myseg = 0;
  if (tid < NSLOT) {
    const bool ok = tid < NWG || sec_valid(tid - NWG);
    f32x2 v = {-INFINITY, 0.f};
    if (ok) v = *(const f32x2*)(a.ml + ((size_t)tid * NB + b) * 2);
    s_m[tid] = v[0]; s_l[tid] = v[1];
    myseg = tid < NWG ? split_of(tid).seg0 : tid - NWG;
  }
  float gp = bf2f(a.x[b * DM + tid]) * bf2f(a.gw[tid]);
#pragma unroll
  for (int o = 32; o > 0; o >>= 1) gp += __shfl_xor(gp, o);
  if ((tid & 63) == 0) s_g[tid >> 6] = gp;
  __syncthreads();
  if (tid < 64) {
    const int pair = tid >> 4, ls = tid & 15;
    const int start = (ls >> 1) * UH + (ls & 1) * UA, end = start + ((ls & 1) ? UB : UA);
    float M = s_m[NWG + tid];
    int rlo = start <= 32 * UE ? (start + UE - 1) / UE : 32 + (start - 32 * UE + UO - 1) / UO;
    int rhi = end <= 32 * UE ? (end + UE - 1) / UE : 32 + (end - 32 * UE + UO - 1) / UO;
    for (int rr = rlo; rr < rhi; ++rr) { const int bid = ((rr & 31) << 3) | (pair << 1) | (rr >> 5); M = fmaxf(M, s_m[bid]); }
    float L = s_l[NWG + tid] > 0.f ? s_l[NWG + tid] * __builtin_amdgcn_exp2f(s_m[NWG + tid] - M) : 0.f;
    for (int rr = rlo; rr < rhi; ++rr) { const int bid = ((rr & 31) << 3) | (pair << 1) | (rr >> 5); L += s_l[bid] * __builtin_amdgcn_exp2f(s_m[bid] - M); }
    float z = 0.f;
#pragma unroll
    for (int k = 0; k < 8; ++k) z += s_g[k];
    const float g = 1.f / (1.f + __expf(-z));
    s_M[tid] = M;
    s_iL[tid] = ((tid & 1) ? 1.f - g : g) / L;
  }
  __syncthreads();
  if (tid < NSLOT) s_w[tid] = s_l[tid] > 0.f ? __builtin_amdgcn_exp2f(s_m[tid] - s_M[myseg]) * s_iL[myseg] : 0.f;
  __syncthreads();
  f32x4 s = {0.f, 0.f, 0.f, 0.f};
#pragma unroll
  for (int k = 0; k < 5; ++k) s += yv[k] * s_w[sg + 64 * k];
  *(f32x4*)(s_red + sg * 32 + ch * 4) = s;
  __syncthreads();
  if (tid < 32) {
    float t = 0.f;
#pragma unroll 8
    for (int k = 0; k < 64; ++k) t += s_red[k * 32 + tid];
    a.out[b * DM + cg * 32 + tid] = f2bf(t);
  }
}

}

extern "C" void kernel_launch(void* const* d_in, const int* in_sizes, int n_in, void* d_out, int out_size, void* d_ws,
                              size_t ws_size, hipStream_t stream) {
  (void)in_sizes; (void)n_in; (void)out_size; (void)ws_size;
  MainArgs a;
  a.x = (const char*)d_in[0];
  a.kA = (const char*)d_in[1]; a.vA = (const char*)d_in[2]; a.wqA = (const char*)d_in[3]; a.woA = (const char*)d_in[4];
  a.kB = (const char*)d_in[6]; a.vB = (const char*)d_in[7]; a.wqB = (const char*)d_in[8]; a.woB = (const char*)d_in[9];
  a.y = (float*)d_ws;
  a.ml = (float*)d_ws + (size_t)NSLOT * 2048;
  hipLaunchKernelGGL(k_main, dim3(NWG), dim3(NTHR), 0, stream, a);
  FinArgs f;
  f.y = a.y; f.ml = a.ml; f.x = (const unsigned short*)d_in[0]; f.gw = (const unsigned short*)d_in[10];
  f.out = (unsigned short*)d_out;
  hipLaunchKernelGGL(k_final, dim3(64), dim3(K2_THR), 0, stream, f);
}
```

```cpp
#include <hip/hip_runtime.h>
#include <hip/hip_bf16.h>
#include <stdint.h>

typedef unsigned u32x4 __attribute__((ext_vector_type(4)));
typedef float f32x4 __attribute__((ext_vector_type(4)));
typedef float f32x2 __attribute__((ext_vector_type(2)));
typedef short bf16x8 __attribute__((ext_vector_type(8)));
typedef __bf16 bf16x2_t __attribute__((ext_vector_type(2)));
typedef unsigned long long u64;

namespace {

constexpr int NB = 4, DM = 512, NH = 32, SK = 4096, DH = 128, WINB = 1024;
constexpr int HD = NH * DH;
constexpr int UA = SK / 16, UB = WINB / 16;
constexpr int UH = UA + UB;
#define K1_UE 40
constexpr int UE = K1_UE, UO = 80 - UE;
static_assert(UE > 0 && UO > 0 && UE <= 60 && UO <= 60, "a workgroup spans at most two segments");
constexpr int NWG = 256, NTHR = 1024;
constexpr int NSLOT = NWG + 64;
constexpr float QSCALE = 0.088388347648318440f * 1.4426950408889634f;

constexpr int L_QP = 0;
constexpr int L_RED = 8192;
constexpr int L_RO = L_RED + 4096;
constexpr int L_RM = L_RO + 2 * 16 * 68;
constexpr int L_RL = L_RM + 32;
constexpr int L_CNT = L_RL + 32;
constexpr int L_TOT = L_CNT + 4;

typedef const __attribute__((address_space(1))) u32x4* gptr4;
typedef const __attribute__((address_space(1))) char* gchar;
__device__ __forceinline__ u32x4 ld_nt(u64 sbase, unsigned voff) { return __builtin_nontemporal_load((gptr4)((gchar)sbase + voff)); }
__device__ __forceinline__ u32x4 ld_df(const char* p) { return *(const u32x4*)p; }
__device__ __forceinline__ float dot2(unsigned k, unsigned q, float acc) {
  return __builtin_amdgcn_fdot2_f32_bf16(__builtin_bit_cast(bf16x2_t, k), __builtin_bit_cast(bf16x2_t, q), acc, false);
}
template <int CTRL> __device__ __forceinline__ float dpp(float x) {
  return __builtin_bit_cast(float, __builtin_amdgcn_mov_dpp(__builtin_bit_cast(int, x), CTRL, 0xf, 0xf, true));
}
__device__ __forceinline__ float row16_sum(float x) {
  x += dpp<0xB1>(x);
  x += dpp<0x4E>(x);
  x += dpp<0x141>(x);
  x += dpp<0x140>(x);
  return x;
}
__device__ __forceinline__ float xrow_max(float x) {
  auto s = __builtin_amdgcn_permlane16_swap(__float_as_uint(x), __float_as_uint(x), false, false);
  x = fmaxf(__uint_as_float(s[0]), __uint_as_float(s[1]));
  auto t = __builtin_amdgcn_permlane32_swap(__float_as_uint(x), __float_as_uint(x), false, false);
  return fmaxf(__uint_as_float(t[0]), __uint_as_float(t[1]));
}
__device__ __forceinline__ float xrow_sum(float x) {
  auto s = __builtin_amdgcn_permlane16_swap(__float_as_uint(x), __float_as_uint(x), false, false);
  x = __uint_as_float(s[0]) + __uint_as_float(s[1]);
  auto t = __builtin_amdgcn_permlane32_swap(__float_as_uint(x), __float_as_uint(x), false, false);
  return __uint_as_float(t[0]) + __uint_as_float(t[1]);
}
__device__ __forceinline__ unsigned short f2bf(float f) { return __builtin_bit_cast(unsigned short, (__bf16)f); }
__device__ __forceinline__ float bf2f(unsigned short h) { return __uint_as_float((unsigned)h << 16); }
__device__ __forceinline__ unsigned pack_bf(float lo, float hi) { return (unsigned)f2bf(lo) | ((unsigned)f2bf(hi) << 16); }

__device__ __forceinline__ float score_of(float d) {
  return bf2f(f2bf(bf2f(f2bf(d)) * 0.088388347648318440f)) * 1.4426950408889634f;
}

struct Split { int seg0, n0, n1, lu0; };
__host__ __device__ __forceinline__ Split split_of(int bid) {
  const int xcd = bid & 7, idx = bid >> 3, pair = xcd >> 1, odd = xcd & 1;
  const int u0 = odd ? 32 * UE + idx * UO : idx * UE, n = odd ? UO : UE;
  const int hh = u0 / UH, rem = u0 - hh * UH, br = rem >= UA ? 1 : 0;
  const int end0 = br ? (hh + 1) * UH : hh * UH + UA;
  Split s;
  s.seg0 = (pair * 8 + hh) * 2 + br;
  s.n0 = (u0 + n < end0 ? u0 + n : end0) - u0;
  s.n1 = n - s.n0;
  s.lu0 = br ? rem - UA : rem;
  return s;
}

__device__ __forceinline__ bf16x8 col_frag(const u32x4 (&W)[8], int e) {
  const unsigned sel = (e & 1) ? 0x07060302u : 0x05040100u;
  u32x4 f;
#pragma unroll
  for (int q = 0; q < 4; ++q) f[q] = __builtin_amdgcn_perm(W[2 * q + 1][e >> 1], W[2 * q][e >> 1], sel);
  return __builtin_bit_cast(bf16x8, f);
}

struct MainArgs {
  const char* x;
  const char* kA; const char* vA; const char* wqA; const char* woA;
  const char* kB; const char* vB; const char* wqB; const char* woB;
  float* y;
  float* ml;
};

#define EXPQ(x) (x)
#define EXPO(x) (x)
#define STAMP(K)
#define STAMPW(K)
__global__ void __launch_bounds__(NTHR) k_main(MainArgs a) {
  __shared__ __attribute__((aligned(16))) float lds[L_TOT];
  STAMP(0)
  const int tid = threadIdx.x, lane = tid & 63;
  const int wave = __builtin_amdgcn_readfirstlane(tid >> 6);
  const int r = lane >> 4, c = lane & 15;
  const int b = wave >> 2, sub = wave & 3;
  const Split sp = split_of(blockIdx.x);
  const int seg0 = sp.seg0, seg1 = sp.seg0 + 1, n0 = sp.n0, n1 = sp.n1;
  const bool mixed = n1 > 0;
  const int h0 = seg0 >> 1, br0 = seg0 & 1, h1 = (seg1 >> 1) & (NH - 1), br1 = seg1 & 1;
  const int g0 = (n0 + 3) >> 2, g1 = (n1 + 3) >> 2;
  const int NG = (g0 + g1 + 1) & ~1;
  const u64 off0 = ((u64)((b * NH + h0) * SK + (br0 ? SK - WINB : 0) + sp.lu0 * 16 + sub * 4)) * 256;
  const u64 off1 = ((u64)((b * NH + h1) * SK + (br1 ? SK - WINB : 0) + sub * 4)) * 256;
  const u64 k0 = (u64)(br0 ? a.kB : a.kA) + off0, v0 = (u64)(br0 ? a.vB : a.vA) + off0;
  const u64 k1 = (u64)(br1 ? a.kB : a.kA) + off1, v1 = (u64)(br1 ? a.vB : a.vA) + off1;
  const unsigned lane16 = lane * 16;

  u32x4 A[8], Bf[8];

#define ISSUE(GI, BUF)                                                                       \
  {                                                                                          \
    const int gi_ = (GI);                                                                    \
    const bool s1_ = gi_ >= g0 && mixed;                                                     \
    const int jb_ = (s1_ ? gi_ - g0 : gi_) * 4, nn_ = s1_ ? n1 : n0;                         \
    const u64 kb_ = s1_ ? k1 : k0, vb_ = s1_ ? v1 : v0;                                      \
    _Pragma("unroll") for (int i_ = 0; i_ < 4; ++i_) {                                       \
      const int j_ = jb_ + i_;                                                               \
      const bool ok_ = j_ < nn_;                                                             \
      const u64 so_ = (u64)(ok_ ? j_ : 0) * 4096;                                            \
      const unsigned vo_ = ok_ ? lane16 : 0u;                                                \
      BUF[i_] = ld_nt(kb_ + so_, vo_);                                                       \
      BUF[4 + i_] = ld_nt(vb_ + so_, vo_);                                                   \
    }                                                                                        \
    __builtin_amdgcn_sched_barrier(0);                                                       \
  }

  const int m16 = lane & 15, g4 = lane >> 4;
  const int sg = wave >> 3, w8 = wave & 7;
  const bool act = sg == 0 || mixed;
  const int brs = sg ? br1 : br0, hs = sg ? h1 : h0;
  unsigned* cnt = (unsigned*)(lds + L_CNT);
  if (tid < 2) cnt[tid] = 0u;
  u32x4 xa0 = {0u, 0u, 0u, 0u}, xa1 = {0u, 0u, 0u, 0u};
  if (act) {
    if (m16 < NB) {
      xa0 = ld_df(a.x + (m16 * DM + w8 * 64 + g4 * 8) * 2);
      xa1 = ld_df(a.x + (m16 * DM + w8 * 64 + 32 + g4 * 8) * 2);
    }
    const char* wq = (brs ? a.wqB : a.wqA) + ((size_t)(w8 * 64 + g4 * 8) * HD + hs * DH + c * 8) * 2;
#pragma unroll
    for (int j = 0; j < 8; ++j) A[j] = ld_df(wq + (size_t)EXPQ(j) * HD * 2);
#pragma unroll
    for (int j = 0; j < 8; ++j) Bf[j] = ld_df(wq + (size_t)EXPQ(32 + j) * HD * 2);
  }
  __builtin_amdgcn_sched_barrier(0);
  asm volatile("s_waitcnt lgkmcnt(0)" ::: "memory");
  __builtin_amdgcn_s_barrier();
  __builtin_amdgcn_sched_barrier(0);
  if (act) {
#pragma unroll
    for (int hf = 0; hf < 2; ++hf) {
      f32x4 d_[4];
#pragma unroll
      for (int e = 0; e < 4; ++e) {
        d_[e] = __builtin_amdgcn_mfma_f32_16x16x32_bf16(__builtin_bit_cast(bf16x8, xa0), col_frag(A, hf * 4 + e), f32x4{0.f, 0.f, 0.f, 0.f}, 0, 0, 0);
        d_[e] = __builtin_amdgcn_mfma_f32_16x16x32_bf16(__builtin_bit_cast(bf16x8, xa1), col_frag(Bf, hf * 4 + e), d_[e], 0, 0, 0);
      }
      if (lane < 16) {
#pragma unroll
        for (int bb = 0; bb < 4; ++bb)
          *(f32x4*)(lds + L_QP + (wave * 4 + bb) * 128 + c * 8 + hf * 4) = f32x4{d_[0][bb], d_[1][bb], d_[2][bb], d_[3][bb]};
      }
    }
  }
  if (act) {
    asm volatile("s_waitcnt lgkmcnt(0)" ::: "memory");
    if (lane == 0) __hip_atomic_fetch_add(cnt + sg, 1u, __ATOMIC_RELAXED, __HIP_MEMORY_SCOPE_WORKGROUP);
  }
  __builtin_amdgcn_sched_barrier(0);
  ISSUE(0, A)
  ISSUE(1, Bf)
#define LOADQ(SEGI)                                                                          \
  {                                                                                          \
    while (__hip_atomic_load(cnt + (SEGI), __ATOMIC_RELAXED, __HIP_MEMORY_SCOPE_WORKGROUP) < 8u) __builtin_amdgcn_s_sleep(1); \
    asm volatile("" ::: "memory");                                                           \
    const float* src_ = lds + L_QP + (((SEGI) * 8 + 2 * r) * 4 + b) * 128 + c * 8;           \
    const f32x4 p0_ = *(const f32x4*)src_ + *(const f32x4*)(src_ + 512);                     \
    const f32x4 p1_ = *(const f32x4*)(src_ + 4) + *(const f32x4*)(src_ + 516);               \
    q[0] = pack_bf(xrow_sum(p0_[0]), xrow_sum(p0_[1]));                                      \
    q[1] = pack_bf(xrow_sum(p0_[2]), xrow_sum(p0_[3]));                                      \
    q[2] = pack_bf(xrow_sum(p1_[0]), xrow_sum(p1_[1]));                                      \
    q[3] = pack_bf(xrow_sum(p1_[2]), xrow_sum(p1_[3]));                                      \
  }
  u32x4 q;
  LOADQ(0)
  STAMP(1)

  float m = -INFINITY, l = 0.f, acc[8];
#pragma unroll
  for (int k = 0; k < 8; ++k) acc[k] = 0.f;

#define FLUSH(SEGI)                                                                          \
  {                                                                                          \
    const float M_ = xrow_max(m);                                                            \
    const float sc_ = (m == -INFINITY) ? 0.f : __builtin_amdgcn_exp2f(m - M_);               \
    const float L_ = xrow_sum(l * sc_);                                                      \
    float o_[8];                                                                             \
    _Pragma("unroll") for (int k = 0; k < 8; ++k) o_[k] = xrow_sum(acc[k] * sc_);            \
    if (lane < 16) {                                                                         \
      *(u32x4*)(lds + L_RO + ((SEGI) * 16 + wave) * 68 + c * 4) =                            \
          u32x4{pack_bf(o_[0], o_[1]), pack_bf(o_[2], o_[3]), pack_bf(o_[4], o_[5]), pack_bf(o_[6], o_[7])}; \
      if (lane == 0) { lds[L_RM + (SEGI) * 16 + wave] = M_; lds[L_RL + (SEGI) * 16 + wave] = L_; } \
    }                                                                                        \
  }

#define COMPUTE(GI, BUF)                                                                     \
  {                                                                                          \
    const int gi_ = (GI);                                                                    \
    if (mixed && gi_ == g0) {                                                                \
      FLUSH(0)                                                                               \
      m = -INFINITY; l = 0.f;                                                                \
      _Pragma("unroll") for (int k = 0; k < 8; ++k) acc[k] = 0.f;                            \
      LOADQ(1)                                                                               \
    }                                                                                        \
    const bool s1_ = gi_ >= g0 && mixed;                                                     \
    const int jb_ = (s1_ ? gi_ - g0 : gi_) * 4, nn_ = s1_ ? n1 : n0;                         \
    float t_[4];                                                                             \
    _Pragma("unroll") for (int i_ = 0; i_ < 4; ++i_) {                                       \
      float d_ = dot2(BUF[i_][0], q[0], 0.f);                                                \
      d_ = dot2(BUF[i_][1], q[1], d_);                                                       \
      d_ = dot2(BUF[i_][2], q[2], d_);                                                       \
      d_ = dot2(BUF[i_][3], q[3], d_);                                                       \
      d_ = score_of(row16_sum(d_));                                                          \
      t_[i_] = (jb_ + i_ < nn_) ? d_ : -INFINITY;                                            \
    }                                                                                        \
    const float mn_ = fmaxf(fmaxf(m, fmaxf(t_[0], t_[1])), fmaxf(t_[2], t_[3]));             \
    const float ms_ = (mn_ == -INFINITY) ? 0.f : mn_;                                        \
    const float al_ = __builtin_amdgcn_exp2f(m - ms_);                                       \
    m = mn_;                                                                                 \
    l *= al_;                                                                                \
    _Pragma("unroll") for (int k = 0; k < 8; ++k) acc[k] *= al_;                             \
    _Pragma("unroll") for (int i_ = 0; i_ < 4; ++i_) {                                       \
      const float p_ = __builtin_amdgcn_exp2f(t_[i_] - ms_);                                 \
      l += p_;                                                                               \
      _Pragma("unroll") for (int k = 0; k < 4; ++k) {                                        \
        const unsigned w_ = BUF[4 + i_][k];                                                  \
        acc[2 * k] = fmaf(p_, __uint_as_float(w_ << 16), acc[2 * k]);                        \
        acc[2 * k + 1] = fmaf(p_, __uint_as_float(w_ & 0xffff0000u), acc[2 * k + 1]);        \
      }                                                                                      \
    }                                                                                        \
    __builtin_amdgcn_sched_barrier(0);                                                       \
  }

  int gi = 0;
#pragma unroll 1
  for (; gi + 2 < NG; gi += 2) {
    COMPUTE(gi, A)
    ISSUE(gi + 2, A)
    COMPUTE(gi + 1, Bf)
    ISSUE(gi + 3, Bf)
  }
  COMPUTE(gi, A)
  STAMP(2)
  COMPUTE(gi + 1, Bf)
#pragma unroll
  for (int k = 0; k < 8; ++k) asm volatile("" : "+v"(acc[k]));
  STAMPW(5)

  const int cb = wave & 3, rp = (wave >> 2) & 1;
  u32x4 F[16];
  if (act) {
    const char* wo = (brs ? a.woB : a.woA) + ((size_t)(hs * DH + rp * 64 + g4 * 8) * DM + cb * 128 + c * 8) * 2;
#pragma unroll
    for (int j = 0; j < 8; ++j) A[j] = ld_df(wo + EXPO(j) * DM * 2);
#pragma unroll
    for (int j = 0; j < 8; ++j) Bf[j] = ld_df(wo + EXPO(32 + j) * DM * 2);
    __builtin_amdgcn_sched_barrier(0);
  }
  {
    const int fs = mixed ? 1 : 0;
    FLUSH(fs)
  }
  __builtin_amdgcn_sched_barrier(0);
#pragma unroll
  for (int e = 0; e < 8; ++e) F[e] = __builtin_bit_cast(u32x4, col_frag(A, e));
  __builtin_amdgcn_sched_barrier(0);
#pragma unroll
  for (int e = 0; e < 8; ++e) F[8 + e] = __builtin_bit_cast(u32x4, col_frag(Bf, e));
  __builtin_amdgcn_sched_barrier(0);
  const int slot = sg == 0 ? (int)blockIdx.x : NWG + seg1;
  int pass = wave < 8 ? 0 : 1;
#pragma unroll 1
  for (; pass < 2; ++pass) {
    if (pass) __syncthreads();
    STAMP(3)
    float yv[8];
    if (act) {
      const u32x4 a0 = *(const u32x4*)(lds + L_RO + (sg * 16 + m16) * 68 + rp * 32 + g4 * 4);
      const u32x4 a1 = *(const u32x4*)(lds + L_RO + (sg * 16 + m16) * 68 + rp * 32 + 16 + g4 * 4);
      const f32x4 mw = *(const f32x4*)(lds + L_RM + sg * 16 + g4 * 4);
      const f32x4 lw = *(const f32x4*)(lds + L_RL + sg * 16 + g4 * 4);
      const float M_ = fmaxf(fmaxf(mw[0], mw[1]), fmaxf(mw[2], mw[3]));
      float sw[4];
#pragma unroll
      for (int w = 0; w < 4; ++w) sw[w] = (mw[w] == -INFINITY) ? 0.f : __builtin_amdgcn_exp2f(mw[w] - M_);
      const float L_ = (sw[0] * lw[0] + sw[1] * lw[1]) + (sw[2] * lw[2] + sw[3] * lw[3]);
      if (pass && (wave & 7) == 0 && c == 0) *(f32x2*)(a.ml + ((size_t)slot * NB + g4) * 2) = f32x2{M_, L_};
#pragma unroll
      for (int e = 0; e < 8; ++e) {
        f32x4 d_ = __builtin_amdgcn_mfma_f32_16x16x32_bf16(__builtin_bit_cast(bf16x8, a0), __builtin_bit_cast(bf16x8, F[e]), f32x4{0.f, 0.f, 0.f, 0.f}, 0, 0, 0);
        d_ = __builtin_amdgcn_mfma_f32_16x16x32_bf16(__builtin_bit_cast(bf16x8, a1), __builtin_bit_cast(bf16x8, F[8 + e]), d_, 0, 0, 0);
        yv[e] = (sw[0] * d_[0] + sw[1] * d_[1]) + (sw[2] * d_[2] + sw[3] * d_[3]);
      }
      if (rp) {
        float* dst = lds + L_RED + sg * 2048 + g4 * 512 + cb * 128 + c * 8;
        *(f32x4*)dst = f32x4{yv[0], yv[1], yv[2], yv[3]};
        *(f32x4*)(dst + 4) = f32x4{yv[4], yv[5], yv[6], yv[7]};
      }
    }
    if (pass) __syncthreads();
    if (act && !rp) {
      const float* src = lds + L_RED + sg * 2048 + g4 * 512 + cb * 128 + c * 8;
      const f32x4 s0 = *(const f32x4*)src + f32x4{yv[0], yv[1], yv[2], yv[3]};
      const f32x4 s1 = *(const f32x4*)(src + 4) + f32x4{yv[4], yv[5], yv[6], yv[7]};
      float* dst = a.y + (size_t)slot * 2048 + g4 * 512 + cb * 128 + c * 8;
      if (pass) { *(f32x4*)dst = s0; *(f32x4*)(dst + 4) = s1; }
    }
  }
  STAMP(4)
}

constexpr int K2_THR = 512;
struct FinArgs {
  const float* y; const float* ml; const unsigned short* x; const unsigned short* gw; unsigned short* out;
};
__device__ __forceinline__ bool sec_valid(int seg) {
  const int ls = seg & 15, start = (ls >> 1) * UH + (ls & 1) * UA;
  return start < 32 * UE ? (start % UE) != 0 : ((start - 32 * UE) % UO) != 0;
}
__global__ void __launch_bounds__(K2_THR) k_final(FinArgs a) {
  __shared__ float s_m[NSLOT], s_l[NSLOT], s_w[NSLOT], s_M[64], s_iL[64], s_g[8];
  __shared__ __attribute__((aligned(16))) float s_red[64 * 32];
  const int tid = threadIdx.x, b = blockIdx.x >> 4, cg = blockIdx.x & 15;
  const int ch = tid & 7, sg = tid >> 3;
  f32x4 yv[5];
#pragma unroll
  for (int k = 0; k < 5; ++k) {
    const int slot = sg + 64 * k;
    const bool ok = k < 4 || sec_valid(sg);
    yv[k] = ok ? *(const f32x4*)(a.y + (size_t)slot * 2048 + b * 512 + cg * 32 + ch * 4) : f32x4{0.f, 0.f, 0.f, 0.f};
  }
  int myseg = 0;
  if (tid < NSLOT) {
    const bool ok = tid < NWG || sec_valid(tid - NWG);
    f32x2 v = {-INFINITY, 0.f};
    if (ok) v = *(const f32x2*)(a.ml + ((size_t)tid * NB + b) * 2);
    s_m[tid] = v[0]; s_l[tid] = v[1];
    myseg = tid < NWG ? split_of(tid).seg0 : tid - NWG;
  }
  float gp = bf2f(a.x[b * DM + tid]) * bf2f(a.gw[tid]);
#pragma unroll
  for (int o = 32; o > 0; o >>= 1) gp += __shfl_xor(gp, o);
  if ((tid & 63) == 0) s_g[tid >> 6] = gp;
  __syncthreads();
  if (tid < 64) {
    const int pair = tid >> 4, ls = tid & 15;
    const int start = (ls >> 1) * UH + (ls & 1) * UA, end = start + ((ls & 1) ? UB : UA);
    float M = s_m[NWG + tid];
    int rlo = start <= 32 * UE ? (start + UE - 1) / UE : 32 + (start - 32 * UE + UO - 1) / UO;
    int rhi = end <= 32 * UE ? (end + UE - 1) / UE : 32 + (end - 32 * UE + UO - 1) / UO;
    for (int rr = rlo; rr < rhi; ++rr) { const int bid = ((rr & 31) << 3) | (pair << 1) | (rr >> 5); M = fmaxf(M, s_m[bid]); }
    float L = s_l[NWG + tid] > 0.f ? s_l[NWG + tid] * __builtin_amdgcn_exp2f(s_m[NWG + tid] - M) : 0.f;
    for (int rr = rlo; rr < rhi; ++rr) { const int bid = ((rr & 31) << 3) | (pair << 1) | (rr >> 5); L += s_l[bid] * __builtin_amdgcn_exp2f(s_m[bid] - M); }
    float z = 0.f;
#pragma unroll
    for (int k = 0; k < 8; ++k) z += s_g[k];
    const float g = 1.f / (1.f + __expf(-z));
    s_M[tid] = M;
    s_iL[tid] = ((tid & 1) ? 1.f - g : g) / L;
  }
  __syncthreads();
  if (tid < NSLOT) s_w[tid] = s_l[tid] > 0.f ? __builtin_amdgcn_exp2f(s_m[tid] - s_M[myseg]) * s_iL[myseg] : 0.f;
  __syncthreads();
  f32x4 s = {0.f, 0.f, 0.f, 0.f};
#pragma unroll
  for (int k = 0; k < 5; ++k) s += yv[k] * s_w[sg + 64 * k];
  *(f32x4*)(s_red + sg * 32 + ch * 4) = s;
  __syncthreads();
  if (tid < 32) {
    float t = 0.f;
#pragma unroll 8
    for (int k = 0; k < 64; ++k) t += s_red[k * 32 + tid];
    a.out[b * DM + cg * 32 + tid] = f2bf(t);
  }
}

}

extern "C" void kernel_launch(void* const* d_in, const int* in_sizes, int n_in, void* d_out, int out_size, void* d_ws,
                              size_t ws_size, hipStream_t stream) {
  (void)in_sizes; (void)n_in; (void)out_size; (void)ws_size;
  MainArgs a;
  a.x = (const char*)d_in[0];
  a.kA = (const char*)d_in[1]; a.vA = (const char*)d_in[2]; a.wqA = (const char*)d_in[3]; a.woA = (const char*)d_in[4];
  a.kB = (const char*)d_in[6]; a.vB = (const char*)d_in[7]; a.wqB = (const char*)d_in[8]; a.woB = (const char*)d_in[9];
  a.y = (float*)d_ws;
  a.ml = (float*)d_ws + (size_t)NSLOT * 2048;
  hipLaunchKernelGGL(k_main, dim3(NWG), dim3(NTHR), 0, stream, a);
  FinArgs f;
  f.y = a.y; f.ml = a.ml; f.x = (const unsigned short*)d_in[0]; f.gw = (const unsigned short*)d_in[10];
  f.out = (unsigned short*)d_out;
  hipLaunchKernelGGL(k_final, dim3(64), dim3(K2_THR), 0, stream, f);
}
```

```cpp
#include <hip/hip_runtime.h>
#include <hip/hip_bf16.h>
#include <stdint.h>
#define POLL_ALL
#define W_DEF

typedef unsigned u32x4 __attribute__((ext_vector_type(4)));
typedef float f32x4 __attribute__((ext_vector_type(4)));
typedef float f32x2 __attribute__((ext_vector_type(2)));
typedef short bf16x8 __attribute__((ext_vector_type(8)));
typedef __bf16 bf16x2_t __attribute__((ext_vector_type(2)));
typedef unsigned long long u64;
#define GAS __attribute__((address_space(1)))
typedef const GAS u32x4* gptr4;
typedef const GAS char* gchar;
typedef GAS u64 gu64;
#define RLX_AGENT __ATOMIC_RELAXED, __HIP_MEMORY_SCOPE_AGENT
#define RLX_WG __ATOMIC_RELAXED, __HIP_MEMORY_SCOPE_WORKGROUP

namespace {

constexpr int DM = 512, NH = 32, SK = 4096, DH = 128, WINB = 1024;
constexpr int HD = NH * DH;
constexpr int NWG = 256, NTHR = 1024;
#define SPINCAP (1u << 17)
constexpr unsigned SPIN_CAP = SPINCAP;

constexpr size_t W_EPOCH = 0;
constexpr size_t W_QG = 64;
constexpr size_t W_OG = W_QG + 32 * 2 * 4 * 128;
constexpr size_t W_ML = W_OG + 32 * 2 * 8 * 4 * 128;
constexpr size_t W_YG = W_ML + 32 * 2 * 64;

constexpr int L_WF = 0;
constexpr int L_RO = L_WF + 8192;
constexpr int L_RM = L_RO + 4096;
constexpr int L_RL = L_RM + 32;
constexpr int L_QP = L_RL + 32;
constexpr int L_QS = L_QP + 2048;
constexpr int L_OF = L_QS + 512;
constexpr int L_FR = L_OF + 272;
constexpr int L_G = L_FR + 512;
constexpr int L_CNT = L_G + 4;
constexpr int L_TOT = L_CNT + 8;

__device__ __forceinline__ u32x4 ld_nt(u64 sbase, unsigned voff) { return __builtin_nontemporal_load((gptr4)((gchar)sbase + voff)); }
__device__ __forceinline__ u32x4 ld_w(const char* p) { return *(const u32x4*)p; }
__device__ __forceinline__ float dot2(unsigned k, unsigned q, float acc) {
  return __builtin_amdgcn_fdot2_f32_bf16(__builtin_bit_cast(bf16x2_t, k), __builtin_bit_cast(bf16x2_t, q), acc, false);
}
template <int CTRL> __device__ __forceinline__ float dpp(float x) {
  return __builtin_bit_cast(float, __builtin_amdgcn_mov_dpp(__builtin_bit_cast(int, x), CTRL, 0xf, 0xf, true));
}
__device__ __forceinline__ float row16_sum(float x) {
  x += dpp<0xB1>(x);
  x += dpp<0x4E>(x);
  x += dpp<0x141>(x);
  x += dpp<0x140>(x);
  return x;
}
__device__ __forceinline__ float x16_sum(float x) {
  auto s = __builtin_amdgcn_permlane16_swap(__float_as_uint(x), __float_as_uint(x), false, false);
  return __uint_as_float(s[0]) + __uint_as_float(s[1]);
}
__device__ __forceinline__ float x32_sum(float x) {
  auto t = __builtin_amdgcn_permlane32_swap(__float_as_uint(x), __float_as_uint(x), false, false);
  return __uint_as_float(t[0]) + __uint_as_float(t[1]);
}
__device__ __forceinline__ float xrow_max(float x) {
  auto s = __builtin_amdgcn_permlane16_swap(__float_as_uint(x), __float_as_uint(x), false, false);
  x = fmaxf(__uint_as_float(s[0]), __uint_as_float(s[1]));
  auto t = __builtin_amdgcn_permlane32_swap(__float_as_uint(x), __float_as_uint(x), false, false);
  return fmaxf(__uint_as_float(t[0]), __uint_as_float(t[1]));
}
__device__ __forceinline__ float xrow_sum(float x) { return x32_sum(x16_sum(x)); }
__device__ __forceinline__ unsigned short f2bf(float f) { return __builtin_bit_cast(unsigned short, (__bf16)f); }
__device__ __forceinline__ float bf2f(unsigned short h) { return __uint_as_float((unsigned)h << 16); }
__device__ __forceinline__ float rbf(float f) { return bf2f(f2bf(f)); }
__device__ __forceinline__ float score_of(float d) { return rbf(rbf(d) * 0.088388347648318440f) * 1.4426950408889634f; }
__device__ __forceinline__ unsigned f2p(float f) {
  const unsigned u = __float_as_uint(f);
  return ((u & 0x7f800000u) == 0x7f800000u ? (u | ((u & 0xffu) ? 0x100u : 0u)) : u + 0x80u) >> 8;
}
__device__ __forceinline__ float p2f(u64 g) { return __uint_as_float((unsigned)g << 8); }

__device__ __forceinline__ bf16x8 col_frag(const u32x4 (&W)[8], int e) {
  const unsigned sel = (e & 1) ? 0x07060302u : 0x05040100u;
  u32x4 f;
#pragma unroll
  for (int q = 0; q < 4; ++q) f[q] = __builtin_amdgcn_perm(W[2 * q + 1][e >> 1], W[2 * q][e >> 1], sel);
  return __builtin_bit_cast(bf16x8, f);
}

__device__ __forceinline__ u64 load_epoch(const u64* p) {
  u64 e;
  asm volatile("s_load_dwordx2 %0, %1, 0x0\n\ts_waitcnt lgkmcnt(0)" : "=&s"(e) : "s"(p) : "memory");
  return e;
}

struct Args {
  const char* x; const char* gw;
  const char* kA; const char* vA; const char* wqA; const char* woA;
  const char* kB; const char* vB; const char* wqB; const char* woB;
  u64* ws; unsigned short* out;
};

#define DBG(TYPE, G, IDX)
#define STAMP(K)
#define STAMPW(K)

__global__ void __launch_bounds__(NTHR) k_fused(Args a) {
  __shared__ __attribute__((aligned(16))) float lds[L_TOT];
  STAMP(0)
  const int tid = threadIdx.x, lane = tid & 63;
  const int wave = __builtin_amdgcn_readfirstlane(tid >> 6);
  const int r = lane >> 4, c = lane & 15;
  const int b = wave & 3, sub = wave >> 2;
  const int h = blockIdx.x >> 3, s = blockIdx.x & 7, pbr = s >> 2, pq = s & 3;
  gu64* ws = (gu64*)a.ws;
  unsigned* cnt = (unsigned*)(lds + L_CNT);
  unsigned short* qs = (unsigned short*)(lds + L_QS);
  const unsigned lane16 = lane * 16;

  if (tid < 8) cnt[tid] = 0u;

  u32x4 A[8], Bf[8];
  u32x4 xa;
  if (wave < 4) {
    xa = ld_w(a.x + ((c & 3) * DM + wave * 128 + (c >> 2) * 32 + r * 8) * 2);
    const char* wq = (pbr ? a.wqB : a.wqA) + ((size_t)(wave * 128 + (c >> 2) * 32 + r * 8) * HD + h * DH + pq * 32 + (c & 3) * 8) * 2;
#pragma unroll
    for (int j = 0; j < 8; ++j) A[j] = ld_w(wq + (size_t)j * HD * 2);
  } else if (wave < 8) {
    const char* wo = (pbr ? a.woB : a.woA) + ((size_t)(h * DH + (wave - 4) * 32 + r * 8) * DM + pq * 128 + c * 8) * 2;
#pragma unroll
    for (int j = 0; j < 8; ++j) A[j] = ld_w(wo + j * DM * 2);
  } else if (wave == 8) {
#pragma unroll
    for (int j = 0; j < 4; ++j) A[j] = ld_w(a.x + j * DM * 2 + lane16);
    A[4] = ld_w(a.gw + lane16);
  }
  __builtin_amdgcn_sched_barrier(0);
  asm volatile("s_waitcnt lgkmcnt(0)" ::: "memory");
  __builtin_amdgcn_s_barrier();
  __builtin_amdgcn_sched_barrier(0);

  if (wave < 4) {
    const u64 tag = (load_epoch(a.ws) + 1ull) << 24;
    __builtin_amdgcn_sched_barrier(0);
#pragma unroll
    for (int hf = 0; hf < 2; ++hf) {
      f32x4 d_[4];
#pragma unroll
      for (int e = 0; e < 4; ++e)
        d_[e] = __builtin_amdgcn_mfma_f32_16x16x32_bf16(__builtin_bit_cast(bf16x8, xa), col_frag(A, hf * 4 + e), f32x4{0.f, 0.f, 0.f, 0.f}, 0, 0, 0);
      if ((c >> 2) == r) {
#pragma unroll
        for (int bb = 0; bb < 4; ++bb)
          *(f32x4*)(lds + L_QP + ((wave * 4 + r) * 4 + bb) * 32 + (c & 3) * 8 + hf * 4) = f32x4{d_[0][bb], d_[1][bb], d_[2][bb], d_[3][bb]};
      }
    }
    asm volatile("s_waitcnt lgkmcnt(0)" ::: "memory");
    STAMP(24)
    if (lane == 0) __hip_atomic_fetch_add(cnt, 1u, RLX_WG);
    while (__hip_atomic_load(cnt, RLX_WG) < 4u) __builtin_amdgcn_s_sleep(1);
    asm volatile("" ::: "memory");
    float qv = 0.f;
#pragma unroll
    for (int p = 0; p < 8; ++p) qv += lds[L_QP + (((lane >> 5) * 8 + p) * 4 + wave) * 32 + (lane & 31)];
    qv = x32_sum(qv);
    if (lane < 32) __hip_atomic_store(ws + W_QG + ((h * 2 + pbr) * 4 + wave) * 128 + pq * 32 + lane, tag | (u64)f2bf(qv), RLX_AGENT);
    STAMP(25)
    u64 g_[4];
    for (unsigned spins = 0; spins < SPIN_CAP; ++spins) {
      bool ok = true;
#pragma unroll
      for (int k = 0; k < 4; ++k) {
        g_[k] = __hip_atomic_load(ws + W_QG + ((h * 2 + (k >> 1)) * 4 + wave) * 128 + (k & 1) * 64 + lane, RLX_AGENT);
        ok &= ((g_[k] ^ tag) >> 24) == 0;
      }
      if (__all(ok)) break;
      __builtin_amdgcn_s_sleep(2);
    }
#pragma unroll
    for (int k = 0; k < 4; ++k) { DBG(1, g_[k], k) }
#pragma unroll
    for (int k = 0; k < 4; ++k) qs[((k >> 1) * 4 + wave) * 128 + (k & 1) * 64 + lane] = (unsigned short)g_[k];
    asm volatile("s_waitcnt lgkmcnt(0)" ::: "memory");
    if (lane == 0) __hip_atomic_store(cnt + 1 + wave, 1u, RLX_WG);
    STAMP(26)
  } else if (wave < 8) {
#pragma unroll
    for (int e = 0; e < 8; ++e) *(bf16x8*)(lds + L_WF + (((wave - 4) * 8 + e) * 64 + lane) * 4) = col_frag(A, e);
  } else if (wave == 8) {
#pragma unroll
    for (int bb = 0; bb < 4; ++bb) {
      float z = 0.f;
#pragma unroll
      for (int k = 0; k < 4; ++k) {
        z += rbf(__uint_as_float(A[bb][k] << 16) * __uint_as_float(A[4][k] << 16));
        z += rbf(__uint_as_float(A[bb][k] & 0xffff0000u) * __uint_as_float(A[4][k] & 0xffff0000u));
      }
      z = rbf(xrow_sum(row16_sum(z)));
      if (lane == 0) lds[L_G + bb] = rbf(1.f / (1.f + __expf(-z)));
    }
  }
  __builtin_amdgcn_sched_barrier(0);

  const u64 rowoff = (u64)(b * NH + h) * SK * 256;
  const u64 kA = (u64)a.kA + rowoff + (u64)(s * 512 + sub * 32) * 256, vA = (u64)a.vA + rowoff + (u64)(s * 512 + sub * 32) * 256;
  const u64 kB = (u64)a.kB + rowoff + (u64)(SK - WINB + s * 128 + sub * 32) * 256, vB = (u64)a.vB + rowoff + (u64)(SK - WINB + s * 128 + sub * 32) * 256;
#define ISSUE(KP, VP, BUF)                                                                   \
  {                                                                                          \
    const u64 kp_ = (KP), vp_ = (VP);                                                        \
    _Pragma("unroll") for (int i_ = 0; i_ < 4; ++i_) BUF[i_] = ld_nt(kp_, lane16 + i_ * 1024); \
    _Pragma("unroll") for (int i_ = 0; i_ < 4; ++i_) BUF[4 + i_] = ld_nt(vp_, lane16 + i_ * 1024); \
    __builtin_amdgcn_sched_barrier(0);                                                       \
  }
  ISSUE(kA, vA, A)
  ISSUE(kA + 4096, vA + 4096, Bf)
  const u64 epoch = load_epoch(a.ws);
  const u64 tag = (epoch + 1ull) << 24;

  while (__hip_atomic_load(cnt + 1 + b, RLX_WG) == 0u) __builtin_amdgcn_s_sleep(2);
  asm volatile("" ::: "memory");
  u32x4 q = *(const u32x4*)(qs + b * 128 + c * 8);
  STAMP(1)

  float m = -INFINITY, l = 0.f;
  f32x2 acc[4];
#pragma unroll
  for (int k = 0; k < 4; ++k) acc[k] = f32x2{0.f, 0.f};

#define FLUSH(SEGI)                                                                          \
  {                                                                                          \
    const float M_ = xrow_max(m);                                                            \
    const float sc_ = (m == -INFINITY) ? 0.f : __builtin_amdgcn_exp2f(m - M_);               \
    const float L_ = xrow_sum(l * sc_);                                                      \
    float o_[8];                                                                             \
    _Pragma("unroll") for (int k = 0; k < 8; ++k) o_[k] = xrow_sum(acc[k >> 1][k & 1] * sc_); \
    if (lane < 16) {                                                                         \
      float* dst_ = lds + L_RO + ((SEGI) * 16 + wave) * 128 + c * 8;                         \
      *(f32x4*)dst_ = f32x4{o_[0], o_[1], o_[2], o_[3]};                                     \
      *(f32x4*)(dst_ + 4) = f32x4{o_[4], o_[5], o_[6], o_[7]};                               \
      if (lane == 0) { lds[L_RM + (SEGI) * 16 + wave] = M_; lds[L_RL + (SEGI) * 16 + wave] = L_; } \
    }                                                                                        \
  }
#define COMPUTE(BUF)                                                                         \
  {                                                                                          \
    float t_[4];                                                                             \
    _Pragma("unroll") for (int i_ = 0; i_ < 4; ++i_) {                                       \
      float d_ = dot2(BUF[i_][0], q[0], 0.f);                                                \
      d_ = dot2(BUF[i_][1], q[1], d_);                                                       \
      d_ = dot2(BUF[i_][2], q[2], d_);                                                       \
      d_ = dot2(BUF[i_][3], q[3], d_);                                                       \
      t_[i_] = score_of(row16_sum(d_));                                                      \
    }                                                                                        \
    const float mn_ = fmaxf(fmaxf(m, fmaxf(t_[0], t_[1])), fmaxf(t_[2], t_[3]));             \
    const float ms_ = (mn_ == -INFINITY) ? 0.f : mn_;                                        \
    const float al_ = __builtin_amdgcn_exp2f(m - ms_);                                       \
    m = mn_;                                                                                 \
    l *= al_;                                                                                \
    _Pragma("unroll") for (int k = 0; k < 4; ++k) acc[k] *= al_;                             \
    _Pragma("unroll") for (int i_ = 0; i_ < 4; ++i_) {                                       \
      const float p_ = __builtin_amdgcn_exp2f(t_[i_] - ms_);                                 \
      l += p_;                                                                               \
      _Pragma("unroll") for (int k = 0; k < 4; ++k) {                                        \
        const unsigned w_ = BUF[4 + i_][k];                                                  \
        const f32x2 v2_ = {__uint_as_float(w_ << 16), __uint_as_float(w_ & 0xffff0000u)};    \
        acc[k] = __builtin_elementwise_fma(f32x2{p_, p_}, v2_, acc[k]);                      \
      }                                                                                      \
    }                                                                                        \
    _Pragma("unroll") for (int k = 0; k < 4; ++k) asm volatile("" : "+v"(acc[k]));   \
    __builtin_amdgcn_sched_barrier(0);                                                       \
  }

#pragma unroll 1
  for (int it = 1; it <= 4; ++it) {
    const u64 kn = it < 4 ? kA + (u64)it * (128 * 256) : kB, vn = it < 4 ? vA + (u64)it * (128 * 256) : vB;
    COMPUTE(A)
    ISSUE(kn, vn, A)
    COMPUTE(Bf)
    ISSUE(kn + 4096, vn + 4096, Bf)
  }
  FLUSH(0)
  m = -INFINITY; l = 0.f;
#pragma unroll
  for (int k = 0; k < 4; ++k) acc[k] = f32x2{0.f, 0.f};
  q = *(const u32x4*)(qs + 512 + b * 128 + c * 8);
  COMPUTE(A)
  COMPUTE(Bf)
  STAMPW(8)
  FLUSH(1)
  __syncthreads();
  STAMP(2)

  {
    const int sg = tid >> 9, bb = (tid >> 7) & 3, d = tid & 127;
    float mw[4], M = -INFINITY;
#pragma unroll
    for (int i = 0; i < 4; ++i) { mw[i] = lds[L_RM + sg * 16 + bb + 4 * i]; M = fmaxf(M, mw[i]); }
    const unsigned Mp = f2p(M);
    const float Mr = __uint_as_float(Mp << 8);
    float o = 0.f, L = 0.f;
#pragma unroll
    for (int i = 0; i < 4; ++i) {
      const float w = (mw[i] == -INFINITY) ? 0.f : __builtin_amdgcn_exp2f(mw[i] - Mr);
      o = fmaf(w, lds[L_RO + (sg * 16 + bb + 4 * i) * 128 + d], o);
      L = fmaf(w, lds[L_RL + sg * 16 + bb + 4 * i], L);
    }
    __hip_atomic_store(ws + W_OG + ((((size_t)h * 2 + sg) * 8 + s) * 4 + bb) * 128 + d, tag | (u64)f2p(o), RLX_AGENT);
    if (d < 2) __hip_atomic_store(ws + W_ML + (h * 2 + sg) * 64 + (s * 4 + bb) * 2 + d, tag | (u64)(d ? f2p(L) : Mp), RLX_AGENT);
  }
  __builtin_amdgcn_s_barrier();
  STAMP(3)

  unsigned short* of = (unsigned short*)(lds + L_OF);
  if (wave < 8) {
    const int bb = wave >> 1, d = (wave & 1) * 64 + lane;
#pragma unroll
    for (int kb = 0; kb < 4; ++kb) A[kb] = *(const u32x4*)(lds + L_WF + ((kb * 8 + wave) * 64 + lane) * 4);
    u64 g_[8], ml;
    for (unsigned spins = 0; spins < SPIN_CAP; ++spins) {
      ml = __hip_atomic_load(ws + W_ML + (h * 2 + pbr) * 64 + lane, RLX_AGENT);
      bool ok = ((ml ^ tag) >> 24) == 0;
#pragma unroll
      for (int j = 0; j < 8; ++j) {
        g_[j] = __hip_atomic_load(ws + W_OG + ((((size_t)h * 2 + pbr) * 8 + j) * 4 + bb) * 128 + d, RLX_AGENT);
        ok &= ((g_[j] ^ tag) >> 24) == 0;
      }
      if (__all(ok)) break;
      __builtin_amdgcn_s_sleep(1);
    }
    DBG(2, ml, 0)
#pragma unroll
    for (int j = 0; j < 8; ++j) { DBG(3, g_[j], j) }
    const float mlf = p2f(ml);
    float Mj[8], Lj[8], M = -INFINITY;
#pragma unroll
    for (int j = 0; j < 8; ++j) {
      Mj[j] = __uint_as_float(__builtin_amdgcn_readlane(__float_as_uint(mlf), (j * 4 + bb) * 2));
      Lj[j] = __uint_as_float(__builtin_amdgcn_readlane(__float_as_uint(mlf), (j * 4 + bb) * 2 + 1));
      M = fmaxf(M, Mj[j]);
    }
    float o = 0.f, L = 0.f;
#pragma unroll
    for (int j = 0; j < 8; ++j) {
      const float w = (Mj[j] == -INFINITY) ? 0.f : __builtin_amdgcn_exp2f(Mj[j] - M);
      o = fmaf(w, p2f(g_[j]), o);
      L = fmaf(w, Lj[j], L);
    }
    of[bb * 136 + d] = f2bf(o / L);
  }
  __syncthreads();
  STAMP(4)
  if (wave < 8) {
    f32x4 d_ = {0.f, 0.f, 0.f, 0.f};
#pragma unroll
    for (int kb = 0; kb < 4; ++kb) {
      u32x4 av = *(const u32x4*)(of + (c & 3) * 136 + kb * 32 + r * 8);
      if (c >= 4) av = u32x4{0u, 0u, 0u, 0u};
      d_ = __builtin_amdgcn_mfma_f32_16x16x32_bf16(__builtin_bit_cast(bf16x8, av), __builtin_bit_cast(bf16x8, A[kb]), d_, 0, 0, 0);
    }
    if (lane < 16) {
#pragma unroll
      for (int bb = 0; bb < 4; ++bb)
        __hip_atomic_store(ws + W_YG + ((size_t)((pq * 8 + wave) * 4 + bb) * 64 + h * 2 + pbr) * 16 + c, tag | (u64)f2p(d_[bb]), RLX_AGENT);
    }
  }
  STAMP(5)

  if ((blockIdx.x & 1) == 0) {
    const int rr = blockIdx.x >> 1;
    u64 g_;
    for (unsigned spins = 0; spins < SPIN_CAP; ++spins) {
      g_ = __hip_atomic_load(ws + W_YG + (size_t)rr * 1024 + tid, RLX_AGENT);
      if (__all(((g_ ^ tag) >> 24) == 0)) break;
      __builtin_amdgcn_s_sleep(1);
    }
    const float v = x32_sum(p2f(g_));
    if (lane < 32) lds[L_FR + wave * 32 + lane] = v;
    __syncthreads();
    STAMP(6)
    if (wave == 0) {
      float t = 0.f;
#pragma unroll
      for (int w = 0; w < 16; ++w) t += lds[L_FR + w * 32 + (lane & 31)];
      const float g = lds[L_G + (rr & 3)];
      t = rbf(rbf(t) * ((lane & 16) ? rbf(1.f - g) : g));
      t = x16_sum(t);
      if (lane < 16) a.out[(rr & 3) * DM + (rr >> 5) * 128 + c * 8 + ((rr >> 2) & 7)] = f2bf(t);
      if (blockIdx.x == 0 && lane == 0) __hip_atomic_store(ws + W_EPOCH, epoch + 1ull, RLX_AGENT);
    }
  }
  STAMP(7)
}

}

extern "C" void kernel_launch(void* const* d_in, const int* in_sizes, int n_in, void* d_out, int out_size, void* d_ws,
                              size_t ws_size, hipStream_t stream) {
  (void)in_sizes; (void)n_in; (void)out_size; (void)ws_size;
  Args a;
  a.x = (const char*)d_in[0]; a.gw = (const char*)d_in[10];
  a.kA = (const char*)d_in[1]; a.vA = (const char*)d_in[2]; a.wqA = (const char*)d_in[3]; a.woA = (const char*)d_in[4];
  a.kB = (const char*)d_in[6]; a.vB = (const char*)d_in[7]; a.wqB = (const char*)d_in[8]; a.woB = (const char*)d_in[9];
  a.ws = (u64*)d_ws; a.out = (unsigned short*)d_out;
  hipLaunchKernelGGL(k_fused, dim3(NWG), dim3(NTHR), 0, stream, a);
}
```

```cpp
#include <hip/hip_runtime.h>
#include <hip/hip_bf16.h>
#include <stdint.h>

typedef unsigned u32x4 __attribute__((ext_vector_type(4)));
typedef float f32x4 __attribute__((ext_vector_type(4)));
typedef float f32x2 __attribute__((ext_vector_type(2)));
typedef short bf16x8 __attribute__((ext_vector_type(8)));
typedef __bf16 bf16x2_t __attribute__((ext_vector_type(2)));
typedef unsigned long long u64;
#define GAS __attribute__((address_space(1)))
typedef const GAS u32x4* gptr4;
typedef const GAS char* gchar;
typedef GAS u64 gu64;
#define RLX_AGENT __ATOMIC_RELAXED, __HIP_MEMORY_SCOPE_AGENT
#define RLX_WG __ATOMIC_RELAXED, __HIP_MEMORY_SCOPE_WORKGROUP

namespace {

constexpr int DM = 512, NH = 32, SK = 4096, DH = 128, WINB = 1024;
constexpr int HD = NH * DH;
constexpr int NWG = 256, NTHR = 1024;
#define SPINCAP (1u << 17)
constexpr unsigned SPIN_CAP = SPINCAP;

constexpr size_t W_EPOCH = 0;
constexpr size_t W_QG = 64;
constexpr size_t W_OG = W_QG + 32 * 2 * 4 * 128;
constexpr size_t W_ML = W_OG + 32 * 2 * 8 * 4 * 128;
constexpr size_t W_YG = W_ML + 32 * 2 * 64;

constexpr int L_WF = 0;
constexpr int L_RO = L_WF + 8192;
constexpr int L_RM = L_RO + 4096;
constexpr int L_RL = L_RM + 32;
constexpr int L_QP = L_RL + 32;
constexpr int L_QS = L_QP + 2048;
constexpr int L_OF = L_QS + 512;
constexpr int L_FR = L_OF + 272;
constexpr int L_G = L_FR + 512;
constexpr int L_CNT = L_G + 4;
constexpr int L_TOT = L_CNT + 8;

__device__ __forceinline__ u32x4 ld_nt(u64 sbase, unsigned voff) { return __builtin_nontemporal_load((gptr4)((gchar)sbase + voff)); }
__device__ __forceinline__ u32x4 ld_w(const char* p) { return *(const u32x4*)p; }
__device__ __forceinline__ float dot2(unsigned k, unsigned q, float acc) {
  return __builtin_amdgcn_fdot2_f32_bf16(__builtin_bit_cast(bf16x2_t, k), __builtin_bit_cast(bf16x2_t, q), acc, false);
}
template <int CTRL> __device__ __forceinline__ float dpp(float x) {
  return __builtin_bit_cast(float, __builtin_amdgcn_mov_dpp(__builtin_bit_cast(int, x), CTRL, 0xf, 0xf, true));
}
__device__ __forceinline__ float row16_sum(float x) {
  x += dpp<0xB1>(x);
  x += dpp<0x4E>(x);
  x += dpp<0x141>(x);
  x += dpp<0x140>(x);
  return x;
}
__device__ __forceinline__ float x16_sum(float x) {
  auto s = __builtin_amdgcn_permlane16_swap(__float_as_uint(x), __float_as_uint(x), false, false);
  return __uint_as_float(s[0]) + __uint_as_float(s[1]);
}
__device__ __forceinline__ float x32_sum(float x) {
  auto t = __builtin_amdgcn_permlane32_swap(__float_as_uint(x), __float_as_uint(x), false, false);
  return __uint_as_float(t[0]) + __uint_as_float(t[1]);
}
__device__ __forceinline__ float xrow_max(float x) {
  auto s = __builtin_amdgcn_permlane16_swap(__float_as_uint(x), __float_as_uint(x), false, false);
  x = fmaxf(__uint_as_float(s[0]), __uint_as_float(s[1]));
  auto t = __builtin_amdgcn_permlane32_swap(__float_as_uint(x), __float_as_uint(x), false, false);
  return fmaxf(__uint_as_float(t[0]), __uint_as_float(t[1]));
}
__device__ __forceinline__ float xrow_sum(float x) { return x32_sum(x16_sum(x)); }
__device__ __forceinline__ unsigned short f2bf(float f) { return __builtin_bit_cast(unsigned short, (__bf16)f); }
__device__ __forceinline__ float bf2f(unsigned short h) { return __uint_as_float((unsigned)h << 16); }
__device__ __forceinline__ float rbf(float f) { return bf2f(f2bf(f)); }
__device__ __forceinline__ float score_of(float d) { return rbf(rbf(d) * 0.088388347648318440f) * 1.4426950408889634f; }
__device__ __forceinline__ unsigned f2p(float f) {
  const unsigned u = __float_as_uint(f);
  return ((u & 0x7f800000u) == 0x7f800000u ? (u | ((u & 0xffu) ? 0x100u : 0u)) : u + 0x80u) >> 8;
}
__device__ __forceinline__ float p2f(u64 g) { return __uint_as_float((unsigned)g << 8); }

__device__ __forceinline__ bf16x8 col_frag(const u32x4 (&W)[8], int e) {
  const unsigned sel = (e & 1) ? 0x07060302u : 0x05040100u;
  u32x4 f;
#pragma unroll
  for (int q = 0; q < 4; ++q) f[q] = __builtin_amdgcn_perm(W[2 * q + 1][e >> 1], W[2 * q][e >> 1], sel);
  return __builtin_bit_cast(bf16x8, f);
}
__device__ __forceinline__ u64 load_epoch(const u64* p) {
  u64 e;
  asm volatile("s_load_dwordx2 %0, %1, 0x0\n\ts_waitcnt lgkmcnt(0)" : "=&s"(e) : "s"(p) : "memory");
  return e;
}

struct Args {
  const char* x; const char* gw;
  const char* kA; const char* vA; const char* wqA; const char* woA;
  const char* kB; const char* vB; const char* wqB; const char* woB;
  u64* ws; unsigned short* out;
};

#define STAMP(K)
#define STAMPW(K)
#define TS(V)

__global__ void __launch_bounds__(NTHR) k_fused(Args a) {
  __shared__ __attribute__((aligned(16))) float lds[L_TOT];
  TS(ts0)
  const int tid = threadIdx.x, lane = tid & 63;
  const int wave = __builtin_amdgcn_readfirstlane(tid >> 6);
  const int r = lane >> 4, c = lane & 15;
  const int b = wave & 3, sub = wave >> 2;
  const int h = blockIdx.x >> 3, s = blockIdx.x & 7, pbr = s >> 2, pq = s & 3;
  gu64* ws = (gu64*)a.ws;
  unsigned* cnt = (unsigned*)(lds + L_CNT);
  unsigned short* qs = (unsigned short*)(lds + L_QS);
  const unsigned lane16 = lane * 16;

  if (tid < 8) cnt[tid] = 0u;
  asm volatile("s_waitcnt lgkmcnt(0)" ::: "memory");
  __builtin_amdgcn_s_barrier();
  __builtin_amdgcn_sched_barrier(0);
  TS(ts1)

  u32x4 A[8], Bf[8];
  const u64 rowoff = (u64)(b * NH + h) * SK * 256;
  const u64 kA = (u64)a.kA + rowoff + (u64)(s * 512 + sub * 32) * 256, vA = (u64)a.vA + rowoff + (u64)(s * 512 + sub * 32) * 256;
  const u64 kB = (u64)a.kB + rowoff + (u64)(SK - WINB + s * 128 + sub * 32) * 256, vB = (u64)a.vB + rowoff + (u64)(SK - WINB + s * 128 + sub * 32) * 256;
#define ISSUE(KP, VP, BUF)                                                                   \
  {                                                                                          \
    const u64 kp_ = (KP), vp_ = (VP);                                                        \
    _Pragma("unroll") for (int i_ = 0; i_ < 4; ++i_) BUF[i_] = ld_nt(kp_, lane16 + i_ * 1024); \
    _Pragma("unroll") for (int i_ = 0; i_ < 4; ++i_) BUF[4 + i_] = ld_nt(vp_, lane16 + i_ * 1024); \
    __builtin_amdgcn_sched_barrier(0);                                                       \
  }
  if (__builtin_expect(wave >= 4, 1)) {
    ISSUE(kA, vA, A)
    ISSUE(kA + 4096, vA + 4096, Bf)
  } else {
    const int qh = (h & ~1) | (s & 1), qq = (s & 2) | (h & 1);
    const u32x4 xa = ld_w(a.x + ((c & 3) * DM + wave * 128 + (c >> 2) * 32 + r * 8) * 2);
    const char* wq = (pbr ? a.wqB : a.wqA) + ((size_t)(wave * 128 + (c >> 2) * 32 + r * 8) * HD + qh * DH + qq * 32 + (c & 3) * 8) * 2;
#pragma unroll
    for (int j = 0; j < 8; ++j) Bf[j] = ld_w(wq + (size_t)j * HD * 2);
    __builtin_amdgcn_sched_barrier(0);
    ISSUE(kA, vA, A)
    const u64 tag = (load_epoch(a.ws) + 1ull) << 24;
    __builtin_amdgcn_sched_barrier(0);
#pragma unroll
    for (int hf = 0; hf < 2; ++hf) {
      f32x4 d_[4];
#pragma unroll
      for (int e = 0; e < 4; ++e)
        d_[e] = __builtin_amdgcn_mfma_f32_16x16x32_bf16(__builtin_bit_cast(bf16x8, xa), col_frag(Bf, hf * 4 + e), f32x4{0.f, 0.f, 0.f, 0.f}, 0, 0, 0);
      if ((c >> 2) == r) {
#pragma unroll
        for (int bb = 0; bb < 4; ++bb)
          *(f32x4*)(lds + L_QP + ((wave * 4 + r) * 4 + bb) * 32 + (c & 3) * 8 + hf * 4) = f32x4{d_[0][bb], d_[1][bb], d_[2][bb], d_[3][bb]};
      }
    }
    asm volatile("s_waitcnt lgkmcnt(0)" ::: "memory");
    STAMP(24)
    if (lane == 0) __hip_atomic_fetch_add(cnt, 1u, RLX_WG);
    while (__hip_atomic_load(cnt, RLX_WG) < 4u) __builtin_amdgcn_s_sleep(1);
    asm volatile("" ::: "memory");
    float qv = 0.f;
#pragma unroll
    for (int p = 0; p < 8; ++p) qv += lds[L_QP + (((lane >> 5) * 8 + p) * 4 + wave) * 32 + (lane & 31)];
    qv = x32_sum(qv);
    if (lane < 32) __hip_atomic_store(ws + W_QG + ((qh * 2 + pbr) * 4 + wave) * 128 + qq * 32 + lane, tag | (u64)f2bf(qv), RLX_AGENT);
    STAMP(25)
    u64 g_[4];
    for (unsigned spins = 0; spins < SPIN_CAP; ++spins) {
      bool ok = true;
#pragma unroll
      for (int k = 0; k < 4; ++k) {
        g_[k] = __hip_atomic_load(ws + W_QG + ((h * 2 + (k >> 1)) * 4 + wave) * 128 + (k & 1) * 64 + lane, RLX_AGENT);
        ok &= ((g_[k] ^ tag) >> 24) == 0;
      }
      if (__all(ok)) break;
      __builtin_amdgcn_s_sleep(2);
    }
#pragma unroll
    for (int k = 0; k < 4; ++k) qs[((k >> 1) * 4 + wave) * 128 + (k & 1) * 64 + lane] = (unsigned short)g_[k];
    asm volatile("s_waitcnt lgkmcnt(0)" ::: "memory");
    if (lane == 0) __hip_atomic_store(cnt + 1 + wave, 1u, RLX_WG);
    STAMP(26)
    __builtin_amdgcn_sched_barrier(0);
    ISSUE(kA + 4096, vA + 4096, Bf)
  }
  TS(ts2)
  const u64 epoch = load_epoch(a.ws);
  const u64 tag = (epoch + 1ull) << 24;
  while (__hip_atomic_load(cnt + 1 + b, RLX_WG) == 0u) __builtin_amdgcn_s_sleep(2);
  asm volatile("" ::: "memory");
  u32x4 q = *(const u32x4*)(qs + b * 128 + c * 8);
  STAMP(1)

  float m = -INFINITY, l = 0.f;
  f32x2 acc[4];
#pragma unroll
  for (int k = 0; k < 4; ++k) acc[k] = f32x2{0.f, 0.f};

#define FLUSH(SEGI)                                                                          \
  {                                                                                          \
    const float M_ = xrow_max(m);                                                            \
    const float sc_ = (m == -INFINITY) ? 0.f : __builtin_amdgcn_exp2f(m - M_);               \
    const float L_ = xrow_sum(l * sc_);                                                      \
    float o_[8];                                                                             \
    _Pragma("unroll") for (int k = 0; k < 8; ++k) o_[k] = xrow_sum(acc[k >> 1][k & 1] * sc_); \
    if (lane < 16) {                                                                         \
      float* dst_ = lds + L_RO + ((SEGI) * 16 + wave) * 128 + c * 8;                         \
      *(f32x4*)dst_ = f32x4{o_[0], o_[1], o_[2], o_[3]};                                     \
      *(f32x4*)(dst_ + 4) = f32x4{o_[4], o_[5], o_[6], o_[7]};                               \
      if (lane == 0) { lds[L_RM + (SEGI) * 16 + wave] = M_; lds[L_RL + (SEGI) * 16 + wave] = L_; } \
    }                                                                                        \
  }
#define COMPUTE(BUF)                                                                         \
  {                                                                                          \
    float t_[4];                                                                             \
    _Pragma("unroll") for (int i_ = 0; i_ < 4; ++i_) {                                       \
      float d_ = dot2(BUF[i_][0], q[0], 0.f);                                                \
      d_ = dot2(BUF[i_][1], q[1], d_);                                                       \
      d_ = dot2(BUF[i_][2], q[2], d_);                                                       \
      d_ = dot2(BUF[i_][3], q[3], d_);                                                       \
      t_[i_] = score_of(row16_sum(d_));                                                      \
    }                                                                                        \
    const float mn_ = fmaxf(fmaxf(m, fmaxf(t_[0], t_[1])), fmaxf(t_[2], t_[3]));             \
    const float ms_ = (mn_ == -INFINITY) ? 0.f : mn_;                                        \
    const float al_ = __builtin_amdgcn_exp2f(m - ms_);                                       \
    m = mn_;                                                                                 \
    l *= al_;                                                                                \
    _Pragma("unroll") for (int k = 0; k < 4; ++k) acc[k] *= al_;                             \
    _Pragma("unroll") for (int i_ = 0; i_ < 4; ++i_) {                                       \
      const float p_ = __builtin_amdgcn_exp2f(t_[i_] - ms_);                                 \
      l += p_;                                                                               \
      _Pragma("unroll") for (int k = 0; k < 4; ++k) {                                        \
        const unsigned w_ = BUF[4 + i_][k];                                                  \
        const f32x2 v2_ = {__uint_as_float(w_ << 16), __uint_as_float(w_ & 0xffff0000u)};    \
        acc[k] = __builtin_elementwise_fma(f32x2{p_, p_}, v2_, acc[k]);                      \
      }                                                                                      \
    }                                                                                        \
    _Pragma("unroll") for (int k = 0; k < 4; ++k) asm volatile("" : "+v"(acc[k]));   \
    __builtin_amdgcn_sched_barrier(0);                                                       \
  }

#pragma unroll 1
  for (int it = 1; it <= 4; ++it) {
    const u64 kn = it < 4 ? kA + (u64)it * (128 * 256) : kB, vn = it < 4 ? vA + (u64)it * (128 * 256) : vB;
    COMPUTE(A)
    ISSUE(kn, vn, A)
    COMPUTE(Bf)
    ISSUE(kn + 4096, vn + 4096, Bf)
  }
  FLUSH(0)
  m = -INFINITY; l = 0.f;
#pragma unroll
  for (int k = 0; k < 4; ++k) acc[k] = f32x2{0.f, 0.f};
  q = *(const u32x4*)(qs + 512 + b * 128 + c * 8);
  COMPUTE(A)
  COMPUTE(Bf)
  STAMPW(8)
#define WO_WAVE0 12
  if (wave >= WO_WAVE0 && wave < WO_WAVE0 + 4) {
    const char* wo = (pbr ? a.woB : a.woA) + ((size_t)(h * DH + (wave - WO_WAVE0) * 32 + r * 8) * DM + pq * 128 + c * 8) * 2;
#pragma unroll
    for (int j = 0; j < 8; ++j) A[j] = ld_w(wo + j * DM * 2);
  } else if (wave == 3) {
#pragma unroll
    for (int j = 0; j < 4; ++j) A[j] = ld_w(a.x + j * DM * 2 + lane16);
    A[4] = ld_w(a.gw + lane16);
#pragma unroll
    for (int bb = 0; bb < 4; ++bb) {
      float z = 0.f;
#pragma unroll
      for (int k = 0; k < 4; ++k) {
        z += rbf(__uint_as_float(A[bb][k] << 16) * __uint_as_float(A[4][k] << 16));
        z += rbf(__uint_as_float(A[bb][k] & 0xffff0000u) * __uint_as_float(A[4][k] & 0xffff0000u));
      }
      z = rbf(xrow_sum(row16_sum(z)));
      if (lane == 0) lds[L_G + bb] = rbf(1.f / (1.f + __expf(-z)));
    }
  }
  __builtin_amdgcn_sched_barrier(0);
  FLUSH(1)
  asm volatile("s_waitcnt lgkmcnt(0)" ::: "memory");
  __builtin_amdgcn_s_barrier();
  asm volatile("" ::: "memory");
  STAMP(2)

  {
    const int sg = tid >> 9, bb = (tid >> 7) & 3, d = tid & 127;
    float mw[4], M = -INFINITY;
#pragma unroll
    for (int i = 0; i < 4; ++i) { mw[i] = lds[L_RM + sg * 16 + bb + 4 * i]; M = fmaxf(M, mw[i]); }
    const unsigned Mp = f2p(M);
    const float Mr = __uint_as_float(Mp << 8);
    float o = 0.f, L = 0.f;
#pragma unroll
    for (int i = 0; i < 4; ++i) {
      const float w = (mw[i] == -INFINITY) ? 0.f : __builtin_amdgcn_exp2f(mw[i] - Mr);
      o = fmaf(w, lds[L_RO + (sg * 16 + bb + 4 * i) * 128 + d], o);
      L = fmaf(w, lds[L_RL + sg * 16 + bb + 4 * i], L);
    }
    __hip_atomic_store(ws + W_OG + ((((size_t)h * 2 + sg) * 8 + s) * 4 + bb) * 128 + d, tag | (u64)f2p(o), RLX_AGENT);
    if (d < 2) __hip_atomic_store(ws + W_ML + (h * 2 + sg) * 64 + (s * 4 + bb) * 2 + d, tag | (u64)(d ? f2p(L) : Mp), RLX_AGENT);
  }
  __builtin_amdgcn_s_barrier();
  STAMP(3)

  unsigned short* of = (unsigned short*)(lds + L_OF);
  if (wave < 8) {
    const int bb = wave >> 1, d = (wave & 1) * 64 + lane;
    u64 g_[8], ml;
    for (unsigned spins = 0; spins < SPIN_CAP; ++spins) {
      ml = __hip_atomic_load(ws + W_ML + (h * 2 + pbr) * 64 + lane, RLX_AGENT);
      bool ok = ((ml ^ tag) >> 24) == 0;
#pragma unroll
      for (int j = 0; j < 8; ++j) {
        g_[j] = __hip_atomic_load(ws + W_OG + ((((size_t)h * 2 + pbr) * 8 + j) * 4 + bb) * 128 + d, RLX_AGENT);
        ok &= ((g_[j] ^ tag) >> 24) == 0;
      }
      if (__all(ok)) break;
      __builtin_amdgcn_s_sleep(1);
    }
    const float mlf = p2f(ml);
    float Mj[8], Lj[8], M = -INFINITY;
#pragma unroll
    for (int j = 0; j < 8; ++j) {
      Mj[j] = __uint_as_float(__builtin_amdgcn_readlane(__float_as_uint(mlf), (j * 4 + bb) * 2));
      Lj[j] = __uint_as_float(__builtin_amdgcn_readlane(__float_as_uint(mlf), (j * 4 + bb) * 2 + 1));
      M = fmaxf(M, Mj[j]);
    }
    float o = 0.f, L = 0.f;
#pragma unroll
    for (int j = 0; j < 8; ++j) {
      const float w = (Mj[j] == -INFINITY) ? 0.f : __builtin_amdgcn_exp2f(Mj[j] - M);
      o = fmaf(w, p2f(g_[j]), o);
      L = fmaf(w, Lj[j], L);
    }
    of[bb * 136 + d] = f2bf(o / L);
  } else if (wave >= WO_WAVE0 && wave < WO_WAVE0 + 4) {
#pragma unroll
    for (int e = 0; e < 8; ++e) *(bf16x8*)(lds + L_WF + (((wave - WO_WAVE0) * 8 + e) * 64 + lane) * 4) = col_frag(A, e);
  }
  __syncthreads();
  STAMP(4)
  if (wave < 8) {
    f32x4 d_ = {0.f, 0.f, 0.f, 0.f};
#pragma unroll
    for (int kb = 0; kb < 4; ++kb) {
      const u32x4 fb = *(const u32x4*)(lds + L_WF + ((kb * 8 + wave) * 64 + lane) * 4);
      u32x4 av = *(const u32x4*)(of + (c & 3) * 136 + kb * 32 + r * 8);
      if (c >= 4) av = u32x4{0u, 0u, 0u, 0u};
      d_ = __builtin_amdgcn_mfma_f32_16x16x32_bf16(__builtin_bit_cast(bf16x8, av), __builtin_bit_cast(bf16x8, fb), d_, 0, 0, 0);
    }
    if (lane < 16) {
#pragma unroll
      for (int bb = 0; bb < 4; ++bb)
        __hip_atomic_store(ws + W_YG + ((size_t)((pq * 8 + wave) * 4 + bb) * 64 + h * 2 + pbr) * 16 + c, tag | (u64)f2p(d_[bb]), RLX_AGENT);
    }
  }
  STAMP(5)

  if ((blockIdx.x & 1) == 0) {
    const int rr = blockIdx.x >> 1;
    u64 g_;
    for (unsigned spins = 0; spins < SPIN_CAP; ++spins) {
      g_ = __hip_atomic_load(ws + W_YG + (size_t)rr * 1024 + tid, RLX_AGENT);
      if (__all(((g_ ^ tag) >> 24) == 0)) break;
      __builtin_amdgcn_s_sleep(1);
    }
    const float v = x32_sum(p2f(g_));
    if (lane < 32) lds[L_FR + wave * 32 + lane] = v;
    __syncthreads();
    if (wave == 0) {
      float t = 0.f;
#pragma unroll
      for (int w = 0; w < 16; ++w) t += lds[L_FR + w * 32 + (lane & 31)];
      const float g = lds[L_G + (rr & 3)];
      t = rbf(rbf(t) * ((lane & 16) ? rbf(1.f - g) : g));
      t = x16_sum(t);
      if (lane < 16) a.out[(rr & 3) * DM + (rr >> 5) * 128 + c * 8 + ((rr >> 2) & 7)] = f2bf(t);
      if (blockIdx.x == 0 && lane == 0) __hip_atomic_store(ws + W_EPOCH, epoch + 1ull, RLX_AGENT);
    }
  }
  STAMP(7)
}

}

extern "C" void kernel_launch(void* const* d_in, const int* in_sizes, int n_in, void* d_out, int out_size, void* d_ws,
                              size_t ws_size, hipStream_t stream) {
  (void)in_sizes; (void)n_in; (void)out_size; (void)ws_size;
  Args a;
  a.x = (const char*)d_in[0]; a.gw = (const char*)d_in[10];
  a.kA = (const char*)d_in[1]; a.vA = (const char*)d_in[2]; a.wqA = (const char*)d_in[3]; a.woA = (const char*)d_in[4];
  a.kB = (const char*)d_in[6]; a.vB = (const char*)d_in[7]; a.wqB = (const char*)d_in[8]; a.woB = (const char*)d_in[9];
  a.ws = (u64*)d_ws; a.out = (unsigned short*)d_out;
  hipLaunchKernelGGL(k_fused, dim3(NWG), dim3(NTHR), 0, stream, a);
}
```

```cpp
#include <hip/hip_runtime.h>
#include <hip/hip_bf16.h>
#include <stdint.h>
#define ROTMUL 1

typedef unsigned u32x4 __attribute__((ext_vector_type(4)));
typedef float f32x4 __attribute__((ext_vector_type(4)));
typedef float f32x2 __attribute__((ext_vector_type(2)));
typedef short bf16x8 __attribute__((ext_vector_type(8)));
typedef __bf16 bf16x2_t __attribute__((ext_vector_type(2)));
typedef unsigned long long u64;
#define GAS __attribute__((address_space(1)))
typedef const GAS u32x4* gptr4;
typedef const GAS char* gchar;
typedef GAS u64 gu64;
#define RLX_AGENT __ATOMIC_RELAXED, __HIP_MEMORY_SCOPE_AGENT
#define RLX_WG __ATOMIC_RELAXED, __HIP_MEMORY_SCOPE_WORKGROUP

namespace {

constexpr int DM = 512, NH = 32, SK = 4096, DH = 128, WINB = 1024;
constexpr int HD = NH * DH;
constexpr int NWG = 256, NTHR = 1024;
#define SPINCAP (1u << 17)
constexpr unsigned SPIN_CAP = SPINCAP;

constexpr size_t W_EPOCH = 0;
constexpr size_t W_QG = 64;
constexpr size_t W_OG = W_QG + 32 * 2 * 4 * 128;
constexpr size_t W_YG = W_OG + 32 * 4 * 144;

constexpr int L_WF = 0;
constexpr int L_FO = L_WF + 32768;
constexpr int L_FM = L_FO + 16 * 68;
constexpr int L_FL = L_FM + 16;
constexpr int L_EO = L_FL + 16;
constexpr int L_EM = L_EO + 1280;
constexpr int L_EL = L_EM + 16;
constexpr int L_QP = L_EL + 16;
constexpr int L_QS = L_QP + 2048;
constexpr int L_G = L_QS + 128;
constexpr int L_CNT = L_G + 4;
constexpr int L_TOT = L_CNT + 8;
constexpr int NA_OWN = 80;

__device__ __forceinline__ u32x4 ld_nt(u64 sbase, unsigned voff) { return __builtin_nontemporal_load((gptr4)((gchar)sbase + voff)); }
__device__ __forceinline__ u32x4 ld_w(const char* p) { return *(const u32x4*)p; }
__device__ __forceinline__ float dot2(unsigned k, unsigned q, float acc) {
  return __builtin_amdgcn_fdot2_f32_bf16(__builtin_bit_cast(bf16x2_t, k), __builtin_bit_cast(bf16x2_t, q), acc, false);
}
template <int CTRL> __device__ __forceinline__ float dpp(float x) {
  return __builtin_bit_cast(float, __builtin_amdgcn_mov_dpp(__builtin_bit_cast(int, x), CTRL, 0xf, 0xf, true));
}
__device__ __forceinline__ float row16_sum(float x) {
  x += dpp<0xB1>(x);
  x += dpp<0x4E>(x);
  x += dpp<0x141>(x);
  x += dpp<0x140>(x);
  return x;
}
__device__ __forceinline__ float x16_sum(float x) {
  auto s = __builtin_amdgcn_permlane16_swap(__float_as_uint(x), __float_as_uint(x), false, false);
  return __uint_as_float(s[0]) + __uint_as_float(s[1]);
}
__device__ __forceinline__ float x32_sum(float x) {
  auto t = __builtin_amdgcn_permlane32_swap(__float_as_uint(x), __float_as_uint(x), false, false);
  return __uint_as_float(t[0]) + __uint_as_float(t[1]);
}
__device__ __forceinline__ float xrow_max(float x) {
  auto s = __builtin_amdgcn_permlane16_swap(__float_as_uint(x), __float_as_uint(x), false, false);
  x = fmaxf(__uint_as_float(s[0]), __uint_as_float(s[1]));
  auto t = __builtin_amdgcn_permlane32_swap(__float_as_uint(x), __float_as_uint(x), false, false);
  return fmaxf(__uint_as_float(t[0]), __uint_as_float(t[1]));
}
__device__ __forceinline__ float xrow_sum(float x) { return x32_sum(x16_sum(x)); }
__device__ __forceinline__ unsigned short f2bf(float f) { return __builtin_bit_cast(unsigned short, (__bf16)f); }
__device__ __forceinline__ float bf2f(unsigned short h) { return __uint_as_float((unsigned)h << 16); }
__device__ __forceinline__ unsigned pack_bf(float lo, float hi) { return (unsigned)f2bf(lo) | ((unsigned)f2bf(hi) << 16); }
__device__ __forceinline__ float rbf(float f) { return bf2f(f2bf(f)); }
__device__ __forceinline__ float score_of(float d) { return rbf(rbf(d) * 0.088388347648318440f) * 1.4426950408889634f; }
__device__ __forceinline__ unsigned f2p(float f) {
  const unsigned u = __float_as_uint(f);
  return ((u & 0x7f800000u) == 0x7f800000u ? (u | ((u & 0xffu) ? 0x100u : 0u)) : u + 0x80u) >> 8;
}
__device__ __forceinline__ float p2f(u64 g) { return __uint_as_float((unsigned)g << 8); }

__device__ __forceinline__ bf16x8 col_frag(const u32x4 (&W)[8], int e) {
  const unsigned sel = (e & 1) ? 0x07060302u : 0x05040100u;
  u32x4 f;
#pragma unroll
  for (int q = 0; q < 4; ++q) f[q] = __builtin_amdgcn_perm(W[2 * q + 1][e >> 1], W[2 * q][e >> 1], sel);
  return __builtin_bit_cast(bf16x8, f);
}
__device__ __forceinline__ u64 load_epoch(const u64* p) {
  u64 e;
  asm volatile("s_load_dwordx2 %0, %1, 0x0\n\ts_waitcnt lgkmcnt(0)" : "=&s"(e) : "s"(p) : "memory");
  return e;
}

struct Args {
  const char* x; const char* gw;
  const char* kA; const char* vA; const char* wqA; const char* woA;
  const char* kB; const char* vB; const char* wqB; const char* woB;
  u64* ws; unsigned short* out;
};

#define STAMP(K)
#define STAMPW(K)
#define TS(V)

__global__ void __launch_bounds__(NTHR) k_fused(Args a) {
  __shared__ __attribute__((aligned(16))) float lds[L_TOT];
  TS(ts0)
  const int tid = threadIdx.x, lane = tid & 63;
  const int wave = __builtin_amdgcn_readfirstlane(tid >> 6);
  const int r = lane >> 4, c = lane & 15;
  const int idx = blockIdx.x >> 3, h = (blockIdx.x & 7) * 4 + (idx >> 3), role = (idx >> 2) & 1, b = idx & 3;
  gu64* ws = (gu64*)a.ws;
  unsigned* cnt = (unsigned*)(lds + L_CNT);
  unsigned short* qs = (unsigned short*)(lds + L_QS);
  const unsigned lane16 = lane * 16;

  if (tid < 8) cnt[tid] = 0u;
  asm volatile("s_waitcnt lgkmcnt(0)" ::: "memory");
  __builtin_amdgcn_s_barrier();
  __builtin_amdgcn_sched_barrier(0);
  TS(ts1)

  u32x4 A[8], Bf[8];
  const u64 rowoff = (u64)(b * NH + h) * SK * 256;
  const int i0 = wave * 5, nA = role ? 128 - NA_OWN : NA_OWN;
  const u64 kA = (u64)a.kA + rowoff + (u64)(role ? NA_OWN : 0) * 8192, vA = (u64)a.vA + rowoff + (u64)(role ? NA_OWN : 0) * 8192;
  const u64 kB = (u64)a.kB + rowoff + (u64)(SK - WINB) * 256, vB = (u64)a.vB + rowoff + (u64)(SK - WINB) * 256;
  const int rotA = ((h * 4 + b) * ROTMUL) % nA, rotB = ((h * 4 + b) * ROTMUL) & 31;
  auto item_off = [&](int p) -> u64 {
    const int pa = p + rotA - (p + rotA >= nA ? nA : 0), pb = (p - nA + rotB) & 31;
    return (u64)(p < nA ? pa : pb) * 8192;
  };
#define KPTR(I) (((I) < nA ? kA : kB) + item_off(I))
#define VPTR(I) (((I) < nA ? vA : vB) + item_off(I))
#define ISSUE(KP, VP, BUF)                                                                   \
  {                                                                                          \
    const u64 kp_ = (KP), vp_ = (VP);                                                        \
    _Pragma("unroll") for (int i_ = 0; i_ < 4; ++i_) BUF[i_] = ld_nt(kp_, lane16 + i_ * 1024); \
    _Pragma("unroll") for (int i_ = 0; i_ < 4; ++i_) BUF[4 + i_] = ld_nt(vp_, lane16 + i_ * 1024); \
    __builtin_amdgcn_sched_barrier(0);                                                       \
  }
  const u64 k0 = KPTR(i0), v0 = VPTR(i0);
  if (__builtin_expect(wave >= 4, 1)) {
    ISSUE(k0, v0, A)
    ISSUE(k0 + 4096, v0 + 4096, Bf)
  } else {
    const u32x4 xa = ld_w(a.x + ((c & 3) * DM + wave * 128 + (c >> 2) * 32 + r * 8) * 2);
    const char* wq = (role ? a.wqB : a.wqA) + ((size_t)(wave * 128 + (c >> 2) * 32 + r * 8) * HD + h * DH + b * 32 + (c & 3) * 8) * 2;
#pragma unroll
    for (int j = 0; j < 8; ++j) Bf[j] = ld_w(wq + (size_t)j * HD * 2);
    __builtin_amdgcn_sched_barrier(0);
    ISSUE(k0, v0, A)
    const u64 ep0 = load_epoch(a.ws);
    const u64 tag = (ep0 + 1ull) << 24;
    __builtin_amdgcn_sched_barrier(0);
#pragma unroll
    for (int hf = 0; hf < 2; ++hf) {
      f32x4 d_[4];
#pragma unroll
      for (int e = 0; e < 4; ++e)
        d_[e] = __builtin_amdgcn_mfma_f32_16x16x32_bf16(__builtin_bit_cast(bf16x8, xa), col_frag(Bf, hf * 4 + e), f32x4{0.f, 0.f, 0.f, 0.f}, 0, 0, 0);
      if ((c >> 2) == r) {
#pragma unroll
        for (int bb = 0; bb < 4; ++bb)
          *(f32x4*)(lds + L_QP + ((wave * 4 + r) * 4 + bb) * 32 + (c & 3) * 8 + hf * 4) = f32x4{d_[0][bb], d_[1][bb], d_[2][bb], d_[3][bb]};
      }
    }
    asm volatile("s_waitcnt lgkmcnt(0)" ::: "memory");
    STAMP(24)
    if (lane == 0) __hip_atomic_fetch_add(cnt, 1u, RLX_WG);
    while (__hip_atomic_load(cnt, RLX_WG) < 4u) __builtin_amdgcn_s_sleep(1);
    asm volatile("" ::: "memory");
    float qv = 0.f;
#pragma unroll
    for (int p = 0; p < 8; ++p) qv += lds[L_QP + (((lane >> 5) * 8 + p) * 4 + wave) * 32 + (lane & 31)];
    qv = x32_sum(qv);
    if (lane < 32) __hip_atomic_store(ws + W_QG + ((h * 2 + role) * 4 + wave) * 128 + b * 32 + lane, tag | (u64)f2bf(qv), RLX_AGENT);
    STAMP(25)
    if (wave == 0) {
      u64 g_[4];
      for (unsigned spins = 0; spins < SPIN_CAP; ++spins) {
        bool ok = true;
#pragma unroll
        for (int k = 0; k < 4; ++k) {
          g_[k] = __hip_atomic_load(ws + W_QG + ((h * 2 + (k >> 1)) * 4 + b) * 128 + (k & 1) * 64 + lane, RLX_AGENT);
          ok &= ((g_[k] ^ tag) >> 24) == 0;
        }
        if (__all(ok)) break;
        __builtin_amdgcn_s_sleep(2);
      }
#pragma unroll
      for (int k = 0; k < 4; ++k) qs[k * 64 + lane] = (unsigned short)g_[k];
      if (lane == 0) *(u64*)(cnt + 4) = ep0;
      asm volatile("s_waitcnt lgkmcnt(0)" ::: "memory");
      if (lane == 0) __hip_atomic_store(cnt + 1, 1u, RLX_WG);
      STAMP(26)
    }
    __builtin_amdgcn_sched_barrier(0);
    ISSUE(k0 + 4096, v0 + 4096, Bf)
  }
  TS(ts2)
  while (__hip_atomic_load(cnt + 1, RLX_WG) == 0u) __builtin_amdgcn_s_sleep(2);
  asm volatile("" ::: "memory");
  const u64 epoch = ((u64)(unsigned)__builtin_amdgcn_readfirstlane(cnt[5]) << 32) | (unsigned)__builtin_amdgcn_readfirstlane(cnt[4]);
  const u64 tag = (epoch + 1ull) << 24;
  u32x4 q = *(const u32x4*)(qs + (i0 < nA ? 0 : 128) + c * 8);
  STAMP(1)

  float m = -INFINITY, l = 0.f;
  f32x2 acc[4];
#pragma unroll
  for (int k = 0; k < 4; ++k) acc[k] = f32x2{0.f, 0.f};

#define ROWMERGE()                                                                           \
    const float M_ = xrow_max(m);                                                            \
    const float sc_ = (m == -INFINITY) ? 0.f : __builtin_amdgcn_exp2f(m - M_);               \
    const float L_ = xrow_sum(l * sc_);                                                      \
    float o_[8];                                                                             \
    _Pragma("unroll") for (int k = 0; k < 8; ++k) o_[k] = xrow_sum(acc[k >> 1][k & 1] * sc_);
#define FLUSH_EXT()                                                                          \
  {                                                                                          \
    ROWMERGE()                                                                               \
    if (lane < 16) {                                                                         \
      float* dst_ = lds + L_EO + wave * 128 + c * 8;                                         \
      *(f32x4*)dst_ = f32x4{o_[0], o_[1], o_[2], o_[3]};                                     \
      *(f32x4*)(dst_ + 4) = f32x4{o_[4], o_[5], o_[6], o_[7]};                               \
      if (lane == 0) { lds[L_EM + wave] = M_; lds[L_EL + wave] = L_; }                       \
    }                                                                                        \
    m = -INFINITY; l = 0.f;                                                                  \
    _Pragma("unroll") for (int k = 0; k < 4; ++k) acc[k] = f32x2{0.f, 0.f};                  \
  }
#define HANDOVER()                                                                           \
  {                                                                                          \
    asm volatile("s_waitcnt lgkmcnt(0)" ::: "memory");                                       \
    unsigned old_ = 0;                                                                       \
    if (lane == 0) old_ = __hip_atomic_fetch_add(cnt + 2, 1u, RLX_WG);                       \
    if (__builtin_amdgcn_readfirstlane(old_) == 9u) {                                        \
      asm volatile("" ::: "memory");                                                         \
      float Mx_ = -INFINITY;                                                                 \
      _Pragma("unroll") for (int w = 0; w < 10; ++w) Mx_ = fmaxf(Mx_, lds[L_EM + w]);        \
      const unsigned Mp_ = f2p(Mx_);                                                         \
      const float Mr_ = __uint_as_float(Mp_ << 8);                                           \
      float e0_ = 0.f, e1_ = 0.f, Ls_ = 0.f;                                                 \
      _Pragma("unroll") for (int w = 0; w < 10; ++w) {                                       \
        const float mw_ = lds[L_EM + w];                                                     \
        const float w_ = (mw_ == -INFINITY) ? 0.f : __builtin_amdgcn_exp2f(mw_ - Mr_);       \
        e0_ = fmaf(w_, lds[L_EO + w * 128 + lane], e0_);                                     \
        e1_ = fmaf(w_, lds[L_EO + w * 128 + 64 + lane], e1_);                                \
        Ls_ = fmaf(w_, lds[L_EL + w], Ls_);                                                  \
      }                                                                                      \
      gu64* og_ = ws + W_OG + (h * 4 + b) * 144;                                             \
      __hip_atomic_store(og_ + lane, tag | (u64)f2p(e0_), RLX_AGENT);                        \
      __hip_atomic_store(og_ + 64 + lane, tag | (u64)f2p(e1_), RLX_AGENT);                   \
      if (lane < 2) __hip_atomic_store(og_ + 128 + lane, tag | (u64)(lane ? f2p(Ls_) : Mp_), RLX_AGENT); \
    }                                                                                        \
  }
#define COMPUTE(BUF)                                                                         \
  {                                                                                          \
    float t_[4];                                                                             \
    _Pragma("unroll") for (int i_ = 0; i_ < 4; ++i_) {                                       \
      float d_ = dot2(BUF[i_][0], q[0], 0.f);                                                \
      d_ = dot2(BUF[i_][1], q[1], d_);                                                       \
      d_ = dot2(BUF[i_][2], q[2], d_);                                                       \
      d_ = dot2(BUF[i_][3], q[3], d_);                                                       \
      t_[i_] = score_of(row16_sum(d_));                                                      \
    }                                                                                        \
    const float mn_ = fmaxf(fmaxf(m, fmaxf(t_[0], t_[1])), fmaxf(t_[2], t_[3]));             \
    const float ms_ = (mn_ == -INFINITY) ? 0.f : mn_;                                        \
    const float al_ = __builtin_amdgcn_exp2f(m - ms_);                                       \
    m = mn_;                                                                                 \
    l *= al_;                                                                                \
    _Pragma("unroll") for (int k = 0; k < 4; ++k) acc[k] *= al_;                             \
    _Pragma("unroll") for (int i_ = 0; i_ < 4; ++i_) {                                       \
      const float p_ = __builtin_amdgcn_exp2f(t_[i_] - ms_);                                 \
      l += p_;                                                                               \
      _Pragma("unroll") for (int k = 0; k < 4; ++k) {                                        \
        const unsigned w_ = BUF[4 + i_][k];                                                  \
        const f32x2 v2_ = {__uint_as_float(w_ << 16), __uint_as_float(w_ & 0xffff0000u)};    \
        acc[k] = __builtin_elementwise_fma(f32x2{p_, p_}, v2_, acc[k]);                      \
      }                                                                                      \
    }                                                                                        \
    _Pragma("unroll") for (int k = 0; k < 4; ++k) asm volatile("" : "+v"(acc[k]));   \
    __builtin_amdgcn_sched_barrier(0);                                                       \
  }

#pragma unroll 1
  for (int it = 1; it <= 4; ++it) {
    const u64 kn = KPTR(i0 + it), vn = VPTR(i0 + it);
    COMPUTE(A)
    ISSUE(kn, vn, A)
    COMPUTE(Bf)
    ISSUE(kn + 4096, vn + 4096, Bf)
    if (i0 + it == nA) {
      FLUSH_EXT()
      HANDOVER()
      q = *(const u32x4*)(qs + 128 + c * 8);
    }
  }
  COMPUTE(A)
  COMPUTE(Bf)
  STAMPW(8)
  if (role && i0 + 5 <= nA) {
    FLUSH_EXT()
    HANDOVER()
  }
  if (!role && wave == 0) {
    FLUSH_EXT()
    const gu64* og = ws + W_OG + (h * 4 + b) * 144;
    u64 g_[8], gm, gl;
    for (unsigned spins = 0; spins < SPIN_CAP; ++spins) {
      gm = __hip_atomic_load(og + 128, RLX_AGENT);
      gl = __hip_atomic_load(og + 129, RLX_AGENT);
      bool ok = (((gm ^ tag) | (gl ^ tag)) >> 24) == 0;
      if (ok) {
#pragma unroll
        for (int k = 0; k < 8; ++k) {
          g_[k] = __hip_atomic_load(og + c * 8 + k, RLX_AGENT);
          ok &= ((g_[k] ^ tag) >> 24) == 0;
        }
        if (__all(ok)) break;
      }
      __builtin_amdgcn_s_sleep(16);
    }
    const float* own = lds + L_EO + c * 8;
    m = r == 0 ? lds[L_EM] : r == 1 ? p2f(gm) : -INFINITY;
    l = r == 0 ? lds[L_EL] : r == 1 ? p2f(gl) : 0.f;
#pragma unroll
    for (int k = 0; k < 8; ++k) acc[k >> 1][k & 1] = r == 0 ? own[k] : r == 1 ? p2f(g_[k]) : 0.f;
  }
  {
    ROWMERGE()
    if (lane < 16) {
      *(u32x4*)(lds + L_FO + wave * 68 + c * 4) = u32x4{pack_bf(o_[0], o_[1]), pack_bf(o_[2], o_[3]), pack_bf(o_[4], o_[5]), pack_bf(o_[6], o_[7])};
      if (lane == 0) { lds[L_FM + wave] = M_; lds[L_FL + wave] = L_; }
    }
  }
  __builtin_amdgcn_sched_barrier(0);
  if (wave >= 1 && wave <= 8) {
    const int cb = (wave - 1) & 3, rp = (wave - 1) >> 2;
    const char* wo = (role ? a.woB : a.woA) + ((size_t)(h * DH + rp * 64 + r * 8) * DM + cb * 128 + c * 8) * 2;
#pragma unroll
    for (int j = 0; j < 8; ++j) A[j] = ld_w(wo + j * DM * 2);
#pragma unroll
    for (int j = 0; j < 8; ++j) Bf[j] = ld_w(wo + (32 + j) * DM * 2);
#pragma unroll
    for (int e = 0; e < 8; ++e) *(bf16x8*)(lds + L_WF + (((cb * 4 + rp * 2) * 8 + e) * 64 + lane) * 4) = col_frag(A, e);
#pragma unroll
    for (int e = 0; e < 8; ++e) *(bf16x8*)(lds + L_WF + (((cb * 4 + rp * 2 + 1) * 8 + e) * 64 + lane) * 4) = col_frag(Bf, e);
    asm volatile("s_waitcnt lgkmcnt(0)" ::: "memory");
    if (lane == 0) __hip_atomic_fetch_add(cnt + 3, 1u, RLX_WG);
  } else if (wave == 9) {
#pragma unroll
    for (int j = 0; j < 4; ++j) A[j] = ld_w(a.x + j * DM * 2 + lane16);
    A[4] = ld_w(a.gw + lane16);
#pragma unroll
    for (int bb = 0; bb < 4; ++bb) {
      float z = 0.f;
#pragma unroll
      for (int k = 0; k < 4; ++k) {
        z += rbf(__uint_as_float(A[bb][k] << 16) * __uint_as_float(A[4][k] << 16));
        z += rbf(__uint_as_float(A[bb][k] & 0xffff0000u) * __uint_as_float(A[4][k] & 0xffff0000u));
      }
      z = rbf(xrow_sum(row16_sum(z)));
      if (lane == 0) lds[L_G + bb] = rbf(1.f / (1.f + __expf(-z)));
    }
  }
  while (__hip_atomic_load(cnt + 3, RLX_WG) < 8u) __builtin_amdgcn_s_sleep(4);
  asm volatile("" ::: "memory");
  const int cb = wave & 3, e0 = (wave >> 2) * 2;
#pragma unroll
  for (int k = 0; k < 8; ++k) A[k] = *(const u32x4*)(lds + L_WF + (((cb * 4 + (k >> 1)) * 8 + e0 + (k & 1)) * 64 + lane) * 4);
  asm volatile("s_waitcnt lgkmcnt(0)" ::: "memory");
  __builtin_amdgcn_s_barrier();
  asm volatile("" ::: "memory");
  STAMP(2)

  {
    u32x4 av[4];
#pragma unroll
    for (int kb = 0; kb < 4; ++kb) av[kb] = *(const u32x4*)(lds + L_FO + c * 68 + kb * 16 + r * 4);
    const f32x4 mw = *(const f32x4*)(lds + L_FM + r * 4), lw = *(const f32x4*)(lds + L_FL + r * 4);
    f32x4 d0 = {0.f, 0.f, 0.f, 0.f}, d1 = {0.f, 0.f, 0.f, 0.f};
#pragma unroll
    for (int kb = 0; kb < 4; ++kb) {
      d0 = __builtin_amdgcn_mfma_f32_16x16x32_bf16(__builtin_bit_cast(bf16x8, av[kb]), __builtin_bit_cast(bf16x8, A[kb * 2]), d0, 0, 0, 0);
      d1 = __builtin_amdgcn_mfma_f32_16x16x32_bf16(__builtin_bit_cast(bf16x8, av[kb]), __builtin_bit_cast(bf16x8, A[kb * 2 + 1]), d1, 0, 0, 0);
    }
    const float M = xrow_max(fmaxf(fmaxf(mw[0], mw[1]), fmaxf(mw[2], mw[3])));
    float sw[4];
#pragma unroll
    for (int k = 0; k < 4; ++k) sw[k] = (mw[k] == -INFINITY) ? 0.f : __builtin_amdgcn_exp2f(mw[k] - M);
    const float L = xrow_sum((sw[0] * lw[0] + sw[1] * lw[1]) + (sw[2] * lw[2] + sw[3] * lw[3]));
    const float y0 = xrow_sum((sw[0] * d0[0] + sw[1] * d0[1]) + (sw[2] * d0[2] + sw[3] * d0[3]));
    const float y1 = xrow_sum((sw[0] * d1[0] + sw[1] * d1[1]) + (sw[2] * d1[2] + sw[3] * d1[3]));
    const float y = (r & 1 ? y1 : y0) / L;
    if (lane < 32) __hip_atomic_store(ws + W_YG + ((size_t)((cb * 8 + e0 + r) * 4 + b) * 64 + h * 2 + role) * 16 + c, tag | (u64)f2p(y), RLX_AGENT);
  }
  __builtin_amdgcn_s_barrier();
  STAMP(5)

  if ((blockIdx.x & 1) == 0) {
    const int rr = blockIdx.x >> 1;
    u64 g_;
    for (unsigned spins = 0; spins < SPIN_CAP; ++spins) {
      g_ = __hip_atomic_load(ws + W_YG + (size_t)rr * 1024 + tid, RLX_AGENT);
      if (__all(((g_ ^ tag) >> 24) == 0)) break;
      __builtin_amdgcn_s_sleep(1);
    }
    const float v = x32_sum(p2f(g_));
    if (lane < 32) lds[L_QP + wave * 32 + lane] = v;
    __syncthreads();
    if (wave == 0) {
      float t = 0.f;
#pragma unroll
      for (int w = 0; w < 16; ++w) t += lds[L_QP + w * 32 + (lane & 31)];
      const float g = lds[L_G + (rr & 3)];
      t = rbf(rbf(t) * ((lane & 16) ? rbf(1.f - g) : g));
      t = x16_sum(t);
      if (lane < 16) a.out[(rr & 3) * DM + (rr >> 5) * 128 + c * 8 + ((rr >> 2) & 7)] = f2bf(t);
      if (blockIdx.x == 0 && lane == 0) __hip_atomic_store(ws + W_EPOCH, epoch + 1ull, RLX_AGENT);
    }
  }
  STAMP(7)
}

}

extern "C" void kernel_launch(void* const* d_in, const int* in_sizes, int n_in, void* d_out, int out_size, void* d_ws,
                              size_t ws_size, hipStream_t stream) {
  (void)in_sizes; (void)n_in; (void)out_size; (void)ws_size;
  Args a;
  a.x = (const char*)d_in[0]; a.gw = (const char*)d_in[10];
  a.kA = (const char*)d_in[1]; a.vA = (const char*)d_in[2]; a.wqA = (const char*)d_in[3]; a.woA = (const char*)d_in[4];
  a.kB = (const char*)d_in[6]; a.vB = (const char*)d_in[7]; a.wqB = (const char*)d_in[8]; a.woB = (const char*)d_in[9];
  a.ws = (u64*)d_ws; a.out = (unsigned short*)d_out;
  hipLaunchKernelGGL(k_fused, dim3(NWG), dim3(NTHR), 0, stream, a);
}
```

```cpp
#include <hip/hip_runtime.h>
#include <hip/hip_bf16.h>
#include <stdint.h>
#define PACE_TAU 3075
#define GATE_POLL

typedef unsigned u32x4 __attribute__((ext_vector_type(4)));
typedef float f32x4 __attribute__((ext_vector_type(4)));
typedef float f32x2 __attribute__((ext_vector_type(2)));
typedef short bf16x8 __attribute__((ext_vector_type(8)));
typedef __bf16 bf16x2_t __attribute__((ext_vector_type(2)));
typedef unsigned long long u64;
#define GAS __attribute__((address_space(1)))
typedef const GAS u32x4* gptr4;
typedef const GAS char* gchar;
typedef GAS u64 gu64;
#define RLX_AGENT __ATOMIC_RELAXED, __HIP_MEMORY_SCOPE_AGENT
#define RLX_WG __ATOMIC_RELAXED, __HIP_MEMORY_SCOPE_WORKGROUP

namespace {

constexpr int DM = 512, NH = 32, SK = 4096, DH = 128, WINB = 1024;
constexpr int HD = NH * DH;
constexpr int NWG = 256, NTHR = 1024;
#define SPINCAP (1u << 17)
constexpr unsigned SPIN_CAP = SPINCAP;

constexpr size_t W_EPOCH = 0;
constexpr size_t W_T0 = 32;
constexpr size_t W_QG = 64;
constexpr size_t W_OG = W_QG + 32 * 2 * 4 * 128;
constexpr size_t W_YG = W_OG + 32 * 4 * 144;

constexpr int L_FO = 0;
constexpr int L_FM = L_FO + 16 * 68;
constexpr int L_FL = L_FM + 16;
constexpr int L_EO = L_FL + 16;
constexpr int L_EM = L_EO + 1280;
constexpr int L_EL = L_EM + 16;
constexpr int L_QP = L_EL + 16;
constexpr int L_QS = L_QP + 2048;
constexpr int L_G = L_QS + 128;
constexpr int L_CNT = L_G + 4;
constexpr int L_WF = L_CNT + 12;
constexpr int L_TOT = L_WF + 32768;
static_assert(L_WF % 4 == 0 && L_CNT % 2 == 0, "alignment");
constexpr int NA_OWN = 80;

__device__ __forceinline__ u32x4 ld_nt(u64 sbase, unsigned voff) { return __builtin_nontemporal_load((gptr4)((gchar)sbase + voff)); }
__device__ __forceinline__ u32x4 ld_w(const char* p) { return *(const u32x4*)p; }
__device__ __forceinline__ float dot2(unsigned k, unsigned q, float acc) {
  return __builtin_amdgcn_fdot2_f32_bf16(__builtin_bit_cast(bf16x2_t, k), __builtin_bit_cast(bf16x2_t, q), acc, false);
}
template <int CTRL> __device__ __forceinline__ float dpp(float x) {
  return __builtin_bit_cast(float, __builtin_amdgcn_mov_dpp(__builtin_bit_cast(int, x), CTRL, 0xf, 0xf, true));
}
__device__ __forceinline__ float row16_sum(float x) {
  x += dpp<0xB1>(x);
  x += dpp<0x4E>(x);
  x += dpp<0x141>(x);
  x += dpp<0x140>(x);
  return x;
}
__device__ __forceinline__ float x16_sum(float x) {
  auto s = __builtin_amdgcn_permlane16_swap(__float_as_uint(x), __float_as_uint(x), false, false);
  return __uint_as_float(s[0]) + __uint_as_float(s[1]);
}
__device__ __forceinline__ float x32_sum(float x) {
  auto t = __builtin_amdgcn_permlane32_swap(__float_as_uint(x), __float_as_uint(x), false, false);
  return __uint_as_float(t[0]) + __uint_as_float(t[1]);
}
__device__ __forceinline__ float xrow_max(float x) {
  auto s = __builtin_amdgcn_permlane16_swap(__float_as_uint(x), __float_as_uint(x), false, false);
  x = fmaxf(__uint_as_float(s[0]), __uint_as_float(s[1]));
  auto t = __builtin_amdgcn_permlane32_swap(__float_as_uint(x), __float_as_uint(x), false, false);
  return fmaxf(__uint_as_float(t[0]), __uint_as_float(t[1]));
}
__device__ __forceinline__ float xrow_sum(float x) { return x32_sum(x16_sum(x)); }
__device__ __forceinline__ float tr32_sum(float a, float b) {
  auto t = __builtin_amdgcn_permlane32_swap(__float_as_uint(a), __float_as_uint(b), false, false);
  return __uint_as_float(t[0]) + __uint_as_float(t[1]);
}
__device__ __forceinline__ float tr16_sum(float a, float b) {
  auto t = __builtin_amdgcn_permlane16_swap(__float_as_uint(a), __float_as_uint(b), false, false);
  return __uint_as_float(t[0]) + __uint_as_float(t[1]);
}
__device__ __forceinline__ unsigned short f2bf(float f) { return __builtin_bit_cast(unsigned short, (__bf16)f); }
__device__ __forceinline__ float bf2f(unsigned short h) { return __uint_as_float((unsigned)h << 16); }
__device__ __forceinline__ unsigned pack_bf(float lo, float hi) { return (unsigned)f2bf(lo) | ((unsigned)f2bf(hi) << 16); }
__device__ __forceinline__ float rbf(float f) { return bf2f(f2bf(f)); }
__device__ __forceinline__ float score_of(float d) { return rbf(rbf(d) * 0.088388347648318440f) * 1.4426950408889634f; }
__device__ __forceinline__ unsigned f2p(float f) {
  const unsigned u = __float_as_uint(f);
  const bool fin = (u & 0x7f800000u) != 0x7f800000u;
  return (fin ? u + 0x80u : (f != f ? u | 0x00400000u : u)) >> 8;
}
__device__ __forceinline__ float p2f(u64 g) { return __uint_as_float((unsigned)g << 8); }

__device__ __forceinline__ bf16x8 col_frag(const u32x4 (&W)[8], int e) {
  const unsigned sel = (e & 1) ? 0x07060302u : 0x05040100u;
  u32x4 f;
#pragma unroll
  for (int q = 0; q < 4; ++q) f[q] = __builtin_amdgcn_perm(W[2 * q + 1][e >> 1], W[2 * q][e >> 1], sel);
  return __builtin_bit_cast(bf16x8, f);
}
__device__ __forceinline__ u64 load_epoch(const u64* p) {
  u64 e;
  asm volatile("s_load_dwordx2 %0, %1, 0x0\n\ts_waitcnt lgkmcnt(0)" : "=&s"(e) : "s"(p) : "memory");
  return e;
}

struct Args {
  const char* x; const char* gw;
  const char* kA; const char* vA; const char* wqA; const char* woA;
  const char* kB; const char* vB; const char* wqB; const char* woB;
  u64* ws; unsigned short* out;
};

#define STAMP(K)
#define STAMPW(K)
#define TS(V)

__global__ void __launch_bounds__(NTHR) k_fused(Args a) {
  __shared__ __attribute__((aligned(16))) float lds[L_TOT];
  TS(ts0)
  const u64 t_start = wall_clock64();
  const int tid = threadIdx.x, lane = tid & 63;
  const int wave = __builtin_amdgcn_readfirstlane(tid >> 6);
  const int r = lane >> 4, c = lane & 15;
  const int idx = blockIdx.x >> 3, h = (blockIdx.x & 7) * 4 + (idx >> 3), role = (idx >> 2) & 1, b = idx & 3;
  gu64* ws = (gu64*)a.ws;
  unsigned* cnt = (unsigned*)(lds + L_CNT);
  unsigned short* qs = (unsigned short*)(lds + L_QS);
  const unsigned lane16 = lane * 16;

  if (tid < 8) cnt[tid] = 0u;
  asm volatile("s_waitcnt lgkmcnt(0)" ::: "memory");
  __builtin_amdgcn_s_barrier();
  __builtin_amdgcn_sched_barrier(0);
  TS(ts1)

  u32x4 A[8], Bf[8];
  const u64 rowoff = (u64)(b * NH + h) * SK * 256;
  const int i0 = wave * 5, nA = role ? 128 - NA_OWN : NA_OWN;
  const u64 kA = (u64)a.kA + rowoff + (u64)(role ? NA_OWN : 0) * 8192, vA = (u64)a.vA + rowoff + (u64)(role ? NA_OWN : 0) * 8192;
  const u64 kB = (u64)a.kB + rowoff + (u64)(SK - WINB) * 256, vB = (u64)a.vB + rowoff + (u64)(SK - WINB) * 256;
#define KPTR(I) ((I) < nA ? kA + (u64)(I) * 8192 : kB + (u64)((I) - nA) * 8192)
#define VPTR(I) ((I) < nA ? vA + (u64)(I) * 8192 : vB + (u64)((I) - nA) * 8192)
#define ISSUE(KP, VP, BUF)                                                                   \
  {                                                                                          \
    const u64 kp_ = (KP), vp_ = (VP);                                                        \
    _Pragma("unroll") for (int i_ = 0; i_ < 4; ++i_) BUF[i_] = ld_nt(kp_, lane16 + i_ * 1024); \
    _Pragma("unroll") for (int i_ = 0; i_ < 4; ++i_) BUF[4 + i_] = ld_nt(vp_, lane16 + i_ * 1024); \
    __builtin_amdgcn_sched_barrier(0);                                                       \
  }
  const u64 k0 = KPTR(i0), v0 = VPTR(i0);
  if (__builtin_expect(wave >= 4, 1)) {
    ISSUE(k0, v0, A)
    ISSUE(k0 + 4096, v0 + 4096, Bf)
  } else {
    const u32x4 xa = ld_w(a.x + ((c & 3) * DM + wave * 128 + (c >> 2) * 32 + r * 8) * 2);
    const char* wq = (role ? a.wqB : a.wqA) + ((size_t)(wave * 128 + (c >> 2) * 32 + r * 8) * HD + h * DH + b * 32 + (c & 3) * 8) * 2;
#pragma unroll
    for (int j = 0; j < 8; ++j) Bf[j] = ld_w(wq + (size_t)j * HD * 2);
    __builtin_amdgcn_sched_barrier(0);
    ISSUE(k0, v0, A)
    const u64 ep0 = load_epoch(a.ws);
    const u64 tag = (ep0 + 1ull) << 24;
    if (blockIdx.x < 8 && wave == 1 && lane == 0) __hip_atomic_store(ws + W_T0 + blockIdx.x, tag | (t_start & 0xffffffull), RLX_AGENT);
    __builtin_amdgcn_sched_barrier(0);
#pragma unroll
    for (int hf = 0; hf < 2; ++hf) {
      f32x4 d_[4];
#pragma unroll
      for (int e = 0; e < 4; ++e)
        d_[e] = __builtin_amdgcn_mfma_f32_16x16x32_bf16(__builtin_bit_cast(bf16x8, xa), col_frag(Bf, hf * 4 + e), f32x4{0.f, 0.f, 0.f, 0.f}, 0, 0, 0);
      if ((c >> 2) == r) {
#pragma unroll
        for (int bb = 0; bb < 4; ++bb)
          *(f32x4*)(lds + L_QP + ((wave * 4 + r) * 4 + bb) * 32 + (c & 3) * 8 + hf * 4) = f32x4{d_[0][bb], d_[1][bb], d_[2][bb], d_[3][bb]};
      }
    }
    asm volatile("s_waitcnt lgkmcnt(0)" ::: "memory");
    if (lane == 0) __hip_atomic_fetch_add(cnt, 1u, RLX_WG);
    while (__hip_atomic_load(cnt, RLX_WG) < 4u) __builtin_amdgcn_s_sleep(1);
    asm volatile("" ::: "memory");
    float qv = 0.f;
#pragma unroll
    for (int p = 0; p < 8; ++p) qv += lds[L_QP + (((lane >> 5) * 8 + p) * 4 + wave) * 32 + (lane & 31)];
    qv = x32_sum(qv);
    if (lane < 32) __hip_atomic_store(ws + W_QG + ((h * 2 + role) * 4 + wave) * 128 + b * 32 + lane, tag | (u64)f2bf(qv), RLX_AGENT);
    if (wave == 0) {
      u64 g_[4];
      for (unsigned spins = 0; spins < SPIN_CAP; ++spins) {
        bool ok = true;
#pragma unroll
        for (int k = 0; k < 4; ++k) {
          g_[k] = __hip_atomic_load(ws + W_QG + ((h * 2 + (k >> 1)) * 4 + b) * 128 + (k & 1) * 64 + lane, RLX_AGENT);
          ok &= ((g_[k] ^ tag) >> 24) == 0;
        }
        if (__all(ok)) break;
        __builtin_amdgcn_s_sleep(2);
      }
#pragma unroll
      for (int k = 0; k < 4; ++k) qs[k * 64 + lane] = (unsigned short)g_[k];
      if (lane == 0) *(u64*)(cnt + 4) = ep0;
      {
        int back = 0;
        const u64 g0 = __hip_atomic_load(ws + W_T0 + (lane & 7), RLX_AGENT);
        const int d = (int)(((unsigned)t_start - (unsigned)g0) << 8) >> 8;
        back = (((g0 ^ tag) >> 24) == 0 && d < 1000) ? d : -100000;
        back = max(back, __shfl_xor(back, 1)); back = max(back, __shfl_xor(back, 2)); back = max(back, __shfl_xor(back, 4));
        if (back == -100000) back = 0;
        if (lane == 0) *(u64*)(cnt + 8) = t_start - (long long)back;
      }
      asm volatile("s_waitcnt lgkmcnt(0)" ::: "memory");
      if (lane == 0) __hip_atomic_store(cnt + 1, 1u, RLX_WG);
      }
    __builtin_amdgcn_sched_barrier(0);
    ISSUE(k0 + 4096, v0 + 4096, Bf)
  }
  TS(ts2)
  while (__hip_atomic_load(cnt + 1, RLX_WG) == 0u) __builtin_amdgcn_s_sleep(2);
  asm volatile("" ::: "memory");
  const u64 epoch = ((u64)(unsigned)__builtin_amdgcn_readfirstlane(cnt[5]) << 32) | (unsigned)__builtin_amdgcn_readfirstlane(cnt[4]);
  const u64 tag = (epoch + 1ull) << 24;
  const u64 t_org = ((u64)(unsigned)__builtin_amdgcn_readfirstlane(cnt[9]) << 32) | (unsigned)__builtin_amdgcn_readfirstlane(cnt[8]);
  u32x4 q = *(const u32x4*)(qs + (i0 < nA ? 0 : 128) + c * 8);

  float m = -INFINITY, l = 0.f;
  f32x2 acc[4];
#pragma unroll
  for (int k = 0; k < 4; ++k) acc[k] = f32x2{0.f, 0.f};

#define ROWMERGE()                                                                           \
    const float L_ = xrow_sum(l);                                                            \
    const float o0_ = tr16_sum(tr32_sum(acc[0][0], acc[2][0]), tr32_sum(acc[1][0], acc[3][0])); \
    const float o1_ = tr16_sum(tr32_sum(acc[0][1], acc[2][1]), tr32_sum(acc[1][1], acc[3][1]));
#define FLUSH_EXT()                                                                          \
  {                                                                                          \
    ROWMERGE()                                                                               \
    *(f32x2*)(lds + L_EO + wave * 128 + c * 8 + r * 2) = f32x2{o0_, o1_};                    \
    if (lane == 0) { lds[L_EM + wave] = m; lds[L_EL + wave] = L_; }                          \
    m = -INFINITY; l = 0.f;                                                                  \
    _Pragma("unroll") for (int k = 0; k < 4; ++k) acc[k] = f32x2{0.f, 0.f};                  \
    asm volatile("s_waitcnt lgkmcnt(0)" ::: "memory");                                       \
    if (lane == 0) __hip_atomic_fetch_add(cnt + 2, 1u, RLX_WG);                              \
  }
#define COMPUTE(BUF)                                                                         \
  {                                                                                          \
    float t_[4];                                                                             \
    _Pragma("unroll") for (int i_ = 0; i_ < 4; ++i_) {                                       \
      float d_ = dot2(BUF[i_][0], q[0], 0.f);                                                \
      d_ = dot2(BUF[i_][1], q[1], d_);                                                       \
      d_ = dot2(BUF[i_][2], q[2], d_);                                                       \
      d_ = dot2(BUF[i_][3], q[3], d_);                                                       \
      t_[i_] = score_of(row16_sum(d_));                                                      \
    }                                                                                        \
    const float mn_ = fmaxf(m, xrow_max(fmaxf(fmaxf(t_[0], t_[1]), fmaxf(t_[2], t_[3]))));   \
    const float ms_ = (mn_ == -INFINITY) ? 0.f : mn_;                                        \
    const float al_ = __builtin_amdgcn_exp2f(m - ms_);                                       \
    m = mn_;                                                                                 \
    l *= al_;                                                                                \
    _Pragma("unroll") for (int k = 0; k < 4; ++k) acc[k] *= al_;                             \
    _Pragma("unroll") for (int i_ = 0; i_ < 4; ++i_) {                                       \
      const float p_ = __builtin_amdgcn_exp2f(t_[i_] - ms_);                                 \
      l += p_;                                                                               \
      _Pragma("unroll") for (int k = 0; k < 4; ++k) {                                        \
        const unsigned w_ = BUF[4 + i_][k];                                                  \
        const f32x2 v2_ = {__uint_as_float(w_ << 16), __uint_as_float(w_ & 0xffff0000u)};    \
        acc[k] = __builtin_elementwise_fma(f32x2{p_, p_}, v2_, acc[k]);                      \
      }                                                                                      \
    }                                                                                        \
    _Pragma("unroll") for (int k = 0; k < 4; ++k) asm volatile("" : "+v"(acc[k]));   \
    __builtin_amdgcn_sched_barrier(0);                                                       \
  }

#define PACE_OFF 0
#define PACE_GAP 2
#define PACE_SLOT(G, R) (32 + (PACE_OFF) + (G) * 32 + ((G) > 2 ? 2 : (G)) * (PACE_GAP) + (R))
#define PACE(K)                                                                              \
  if (PACE_TAU > 0) {                                                                        \
    const long long due_ = (long long)(PACE_SLOT(wave >> 2, (K) * 4 + (wave & 3)) * PACE_TAU) / 100; \
    for (int n_ = 0; n_ < 4000 && (long long)(wall_clock64() - t_org) < due_; ++n_) __builtin_amdgcn_s_sleep(2); \
    __builtin_amdgcn_sched_barrier(0);                                                       \
  }
#pragma unroll 1
  for (int it = 1; it <= 4; ++it) {
    const u64 kn = KPTR(i0 + it), vn = VPTR(i0 + it);
    COMPUTE(A)
    PACE(2 * it - 2)
    ISSUE(kn, vn, A)
    COMPUTE(Bf)
    PACE(2 * it - 1)
    ISSUE(kn + 4096, vn + 4096, Bf)
    if (i0 + it == nA) {
      FLUSH_EXT()
      q = *(const u32x4*)(qs + 128 + c * 8);
    }
  }
  COMPUTE(A)
  COMPUTE(Bf)
  TS(ta)
  if (lane == 0) __hip_atomic_fetch_add(cnt + 6, 1u, RLX_WG);
  if (role && i0 + 5 <= nA) {
    FLUSH_EXT()
  }
  if (wave < 8) {
    const int cb = wave & 3, rp = wave >> 2;
    const char* wo = (role ? a.woB : a.woA) + ((size_t)(h * DH + rp * 64 + r * 8) * DM + cb * 128 + c * 8) * 2;
#define WO_STEP 4
#pragma unroll 1
    for (int hf = 0; hf < 2; ++hf) {
      if (PACE_TAU > 0 && WO_STEP > 0) {
        const long long due_ = (long long)((68 + (PACE_OFF) + (wave * 2 + hf) * WO_STEP) * PACE_TAU) / 100;
        for (int n_ = 0; n_ < 4000 && (long long)(wall_clock64() - t_org) < due_; ++n_) __builtin_amdgcn_s_sleep(4);
      }
#pragma unroll
      for (int j = 0; j < 8; ++j) A[j] = ld_w(wo + (hf * 32 + j) * DM * 2);
#pragma unroll
      for (int e = 0; e < 8; ++e) *(bf16x8*)(lds + L_WF + (((cb * 4 + rp * 2 + hf) * 8 + e) * 64 + lane) * 4) = col_frag(A, e);
    }
    asm volatile("s_waitcnt lgkmcnt(0)" ::: "memory");
    if (lane == 0) __hip_atomic_fetch_add(cnt + 3, 1u, RLX_WG);
  } else if (wave == 8) {
#pragma unroll
    for (int j = 0; j < 4; ++j) A[j] = ld_w(a.x + j * DM * 2 + lane16);
    A[4] = ld_w(a.gw + lane16);
#pragma unroll
    for (int bb = 0; bb < 4; ++bb) {
      float z = 0.f;
#pragma unroll
      for (int k = 0; k < 4; ++k) {
        z += rbf(__uint_as_float(A[bb][k] << 16) * __uint_as_float(A[4][k] << 16));
        z += rbf(__uint_as_float(A[bb][k] & 0xffff0000u) * __uint_as_float(A[4][k] & 0xffff0000u));
      }
      z = rbf(xrow_sum(row16_sum(z)));
      if (lane == 0) lds[L_G + bb] = rbf(1.f / (1.f + __expf(-z)));
    }
  }
  __builtin_amdgcn_sched_barrier(0);
  if (role && wave == 0) {
    while (__hip_atomic_load(cnt + 2, RLX_WG) < 10u) __builtin_amdgcn_s_sleep(8);
    asm volatile("" ::: "memory");
    float Mx = -INFINITY;
#pragma unroll
    for (int w = 0; w < 10; ++w) Mx = fmaxf(Mx, lds[L_EM + w]);
    const unsigned Mp = f2p(Mx);
    const float Mr = __uint_as_float(Mp << 8);
    float e0_ = 0.f, e1_ = 0.f, Ls = 0.f;
#pragma unroll
    for (int w = 0; w < 10; ++w) {
      const float mw_ = lds[L_EM + w];
      const float w_ = (mw_ == -INFINITY) ? 0.f : __builtin_amdgcn_exp2f(mw_ - Mr);
      e0_ = fmaf(w_, lds[L_EO + w * 128 + lane], e0_);
      e1_ = fmaf(w_, lds[L_EO + w * 128 + 64 + lane], e1_);
      Ls = fmaf(w_, lds[L_EL + w], Ls);
    }
    gu64* og = ws + W_OG + (h * 4 + b) * 144;
    __hip_atomic_store(og + lane, tag | (u64)f2p(e0_), RLX_AGENT);
    __hip_atomic_store(og + 64 + lane, tag | (u64)f2p(e1_), RLX_AGENT);
    if (lane < 2) __hip_atomic_store(og + 128 + lane, tag | (u64)(lane ? f2p(Ls) : Mp), RLX_AGENT);
  }
  if (!role && wave == 0) {
    const gu64* og = ws + W_OG + (h * 4 + b) * 144;
    u64 g_[8], gm, gl;
    while (__hip_atomic_load(cnt + 6, RLX_WG) < 12u) __builtin_amdgcn_s_sleep(16);
    for (unsigned spins = 0; spins < SPIN_CAP; ++spins) {
      gm = __hip_atomic_load(og + 128, RLX_AGENT);
      gl = __hip_atomic_load(og + 129, RLX_AGENT);
      bool ok = (((gm ^ tag) | (gl ^ tag)) >> 24) == 0;
      if (ok) {
#pragma unroll
        for (int k = 0; k < 8; ++k) {
          g_[k] = __hip_atomic_load(og + c * 8 + k, RLX_AGENT);
          ok &= ((g_[k] ^ tag) >> 24) == 0;
        }
        if (__all(ok)) break;
      }
      __builtin_amdgcn_s_sleep(16);
    }
    const float mh = p2f(gm), mn = fmaxf(m, mh), ms = (mn == -INFINITY) ? 0.f : mn;
    const float wo_ = __builtin_amdgcn_exp2f(m - ms), wh = r == 0 ? __builtin_amdgcn_exp2f(mh - ms) : 0.f;
    m = mn;
    l = fmaf(wh, p2f(gl), l * wo_);
#pragma unroll
    for (int k = 0; k < 8; ++k) acc[k >> 1][k & 1] = fmaf(wh, p2f(g_[k]), acc[k >> 1][k & 1] * wo_);
  }
  {
    ROWMERGE()
    *(unsigned*)(lds + L_FO + wave * 68 + c * 4 + r) = pack_bf(o0_, o1_);
    if (lane == 0) { lds[L_FM + wave] = m; lds[L_FL + wave] = L_; }
  }
  TS(tb)
  if (wave >= 8) {
    asm volatile("s_waitcnt lgkmcnt(0)" ::: "memory");
    __builtin_amdgcn_s_barrier();
  } else {
    const int cb = wave & 3, e0 = (wave >> 2) * 4;
    gu64* ydst = ws + W_YG + ((size_t)((cb * 8 + e0 + r) * 4 + b) * 64 + h * 2 + role) * 16 + c;
    while (__hip_atomic_load(cnt + 3, RLX_WG) < 8u) __builtin_amdgcn_s_sleep(4);
    asm volatile("" ::: "memory");
#pragma unroll
    for (int k = 0; k < 8; ++k) {
      A[k] = *(const u32x4*)(lds + L_WF + (((cb * 4 + (k >> 1)) * 8 + e0 + (k & 1)) * 64 + lane) * 4);
      Bf[k] = *(const u32x4*)(lds + L_WF + (((cb * 4 + (k >> 1)) * 8 + e0 + 2 + (k & 1)) * 64 + lane) * 4);
    }
    asm volatile("" : "+v"(ydst));
    asm volatile("s_waitcnt lgkmcnt(0)" ::: "memory");
    __builtin_amdgcn_s_barrier();
    asm volatile("" ::: "memory");
    u32x4 av[4];
#pragma unroll
    for (int kb = 0; kb < 4; ++kb) av[kb] = *(const u32x4*)(lds + L_FO + c * 68 + kb * 16 + r * 4);
    const f32x4 mw = *(const f32x4*)(lds + L_FM + r * 4), lw = *(const f32x4*)(lds + L_FL + r * 4);
    f32x4 d[4];
#pragma unroll
    for (int e = 0; e < 4; ++e) d[e] = f32x4{0.f, 0.f, 0.f, 0.f};
#pragma unroll
    for (int kb = 0; kb < 4; ++kb) {
      d[0] = __builtin_amdgcn_mfma_f32_16x16x32_bf16(__builtin_bit_cast(bf16x8, av[kb]), __builtin_bit_cast(bf16x8, A[kb * 2]), d[0], 0, 0, 0);
      d[1] = __builtin_amdgcn_mfma_f32_16x16x32_bf16(__builtin_bit_cast(bf16x8, av[kb]), __builtin_bit_cast(bf16x8, A[kb * 2 + 1]), d[1], 0, 0, 0);
      d[2] = __builtin_amdgcn_mfma_f32_16x16x32_bf16(__builtin_bit_cast(bf16x8, av[kb]), __builtin_bit_cast(bf16x8, Bf[kb * 2]), d[2], 0, 0, 0);
      d[3] = __builtin_amdgcn_mfma_f32_16x16x32_bf16(__builtin_bit_cast(bf16x8, av[kb]), __builtin_bit_cast(bf16x8, Bf[kb * 2 + 1]), d[3], 0, 0, 0);
    }
    const float M = xrow_max(fmaxf(fmaxf(mw[0], mw[1]), fmaxf(mw[2], mw[3])));
    float sw[4];
#pragma unroll
    for (int k = 0; k < 4; ++k) sw[k] = (mw[k] == -INFINITY) ? 0.f : __builtin_amdgcn_exp2f(mw[k] - M);
    const float L = xrow_sum((sw[0] * lw[0] + sw[1] * lw[1]) + (sw[2] * lw[2] + sw[3] * lw[3]));
    float ye[4];
#pragma unroll
    for (int e = 0; e < 4; ++e) ye[e] = (sw[0] * d[e][0] + sw[1] * d[e][1]) + (sw[2] * d[e][2] + sw[3] * d[e][3]);
    const float y = tr16_sum(tr32_sum(ye[0], ye[2]), tr32_sum(ye[1], ye[3])) * __builtin_amdgcn_rcpf(L);
    __hip_atomic_store(ydst, tag | (u64)f2p(y), RLX_AGENT);
  }
  TS(te)
  __builtin_amdgcn_s_barrier();
  TS(tf)

  if ((blockIdx.x & 1) == 0) {
    const int rr = blockIdx.x >> 1;
    u64 g_;
#define FIN_DELAY 190
    { const long long due_ = (long long)(PACE_SLOT(3, 31) * PACE_TAU) / 100 + FIN_DELAY;
      for (int n_ = 0; n_ < 4000 && (long long)(wall_clock64() - t_org) < due_; ++n_) __builtin_amdgcn_s_sleep(1); }
    for (unsigned spins = 0; spins < SPIN_CAP; ++spins) {
      g_ = __hip_atomic_load(ws + W_YG + (size_t)rr * 1024 + tid, RLX_AGENT);
      if (__all(((g_ ^ tag) >> 24) == 0)) break;
#define FIN_SLEEP 1
      __builtin_amdgcn_s_sleep(FIN_SLEEP);
    }
    const float v = x32_sum(p2f(g_));
    if (lane < 32) lds[L_QP + lane * 20 + wave] = v;
    __syncthreads();
    if (wave == 0) {
      const float* src = lds + L_QP + (lane & 31) * 20 + (lane >> 5) * 8;
      const f32x4 u = *(const f32x4*)src + *(const f32x4*)(src + 4);
      float t = x32_sum((u[0] + u[1]) + (u[2] + u[3]));
      const float g = lds[L_G + (rr & 3)];
      t = rbf(rbf(t) * ((lane & 16) ? rbf(1.f - g) : g));
      t = x16_sum(t);
      if (lane < 16) a.out[(rr & 3) * DM + (rr >> 5) * 128 + c * 8 + ((rr >> 2) & 7)] = f2bf(t);
      if (blockIdx.x == 0 && lane == 0) __hip_atomic_store(ws + W_EPOCH, epoch + 1ull, RLX_AGENT);
    }
  }
  TS(ti)
}

}

extern "C" void kernel_launch(void* const* d_in, const int* in_sizes, int n_in, void* d_out, int out_size, void* d_ws,
                              size_t ws_size, hipStream_t stream) {
  (void)in_sizes; (void)n_in; (void)out_size; (void)ws_size;
  Args a;
  a.x = (const char*)d_in[0]; a.gw = (const char*)d_in[10];
  a.kA = (const char*)d_in[1]; a.vA = (const char*)d_in[2]; a.wqA = (const char*)d_in[3]; a.woA = (const char*)d_in[4];
  a.kB = (const char*)d_in[6]; a.vB = (const char*)d_in[7]; a.wqB = (const char*)d_in[8]; a.woB = (const char*)d_in[9];
  a.ws = (u64*)d_ws; a.out = (unsigned short*)d_out;
  hipLaunchKernelGGL(k_fused, dim3(NWG), dim3(NTHR), 0, stream, a);
}
```

```cpp
#include <hip/hip_runtime.h>
#include <hip/hip_bf16.h>
#include <stdint.h>

typedef unsigned u32x4 __attribute__((ext_vector_type(4)));
typedef float f32x4 __attribute__((ext_vector_type(4)));
typedef float f32x2 __attribute__((ext_vector_type(2)));
typedef short bf16x8 __attribute__((ext_vector_type(8)));
typedef __bf16 bf16x2_t __attribute__((ext_vector_type(2)));
typedef unsigned long long u64;
#define GAS __attribute__((address_space(1)))
typedef const GAS u32x4* gptr4;
typedef const GAS char* gchar;
typedef GAS u64 gu64;
#define RLX_AGENT __ATOMIC_RELAXED, __HIP_MEMORY_SCOPE_AGENT
#define RLX_WG __ATOMIC_RELAXED, __HIP_MEMORY_SCOPE_WORKGROUP

#define PACE_TAU 3060
#define PACE_OFF 7
#define FIN_DELAY 205

namespace {

constexpr int DM = 512, NH = 32, SK = 4096, DH = 128, WINB = 1024;
constexpr int HD = NH * DH;
constexpr int NWG = 256, NTHR = 1024;
constexpr int NA_OWN = 80;
constexpr unsigned SPIN_CAP = 1u << 17;

constexpr size_t W_EPOCH = 0;
constexpr size_t W_T0 = 32;
constexpr size_t W_QG = 64;
constexpr size_t W_OG = W_QG + 32 * 2 * 4 * 128;
constexpr size_t W_YG = W_OG + 32 * 4 * 144;

constexpr int L_FO = 0;
constexpr int L_FM = L_FO + 16 * 68;
constexpr int L_FL = L_FM + 16;
constexpr int L_EO = L_FL + 16;
constexpr int L_EM = L_EO + 1280;
constexpr int L_EL = L_EM + 16;
constexpr int L_QP = L_EL + 16;
constexpr int L_QS = L_QP + 2048;
constexpr int L_G = L_QS + 128;
constexpr int L_CNT = L_G + 4;
constexpr int L_WF = L_CNT + 12;
constexpr int L_TOT = L_WF + 32768;
static_assert(L_WF % 4 == 0 && L_CNT % 2 == 0 && L_QP % 4 == 0 && L_FM % 4 == 0 && L_QS % 4 == 0, "alignment");

__device__ __forceinline__ u32x4 ld_nt(u64 sbase, unsigned voff) { return __builtin_nontemporal_load((gptr4)((gchar)sbase + voff)); }
__device__ __forceinline__ u32x4 ld_w(const char* p) { return *(const u32x4*)p; }
__device__ __forceinline__ float dot2(unsigned k, unsigned q, float acc) {
  return __builtin_amdgcn_fdot2_f32_bf16(__builtin_bit_cast(bf16x2_t, k), __builtin_bit_cast(bf16x2_t, q), acc, false);
}
template <int CTRL> __device__ __forceinline__ float dpp(float x) {
  return __builtin_bit_cast(float, __builtin_amdgcn_mov_dpp(__builtin_bit_cast(int, x), CTRL, 0xf, 0xf, true));
}
__device__ __forceinline__ float row16_sum(float x) {
  x += dpp<0xB1>(x);
  x += dpp<0x4E>(x);
  x += dpp<0x141>(x);
  x += dpp<0x140>(x);
  return x;
}
__device__ __forceinline__ float tr32_sum(float a, float b) {
  auto t = __builtin_amdgcn_permlane32_swap(__float_as_uint(a), __float_as_uint(b), false, false);
  return __uint_as_float(t[0]) + __uint_as_float(t[1]);
}
__device__ __forceinline__ float tr16_sum(float a, float b) {
  auto t = __builtin_amdgcn_permlane16_swap(__float_as_uint(a), __float_as_uint(b), false, false);
  return __uint_as_float(t[0]) + __uint_as_float(t[1]);
}
__device__ __forceinline__ float x16_sum(float x) { return tr16_sum(x, x); }
__device__ __forceinline__ float x32_sum(float x) { return tr32_sum(x, x); }
__device__ __forceinline__ float xrow_sum(float x) { return x32_sum(x16_sum(x)); }
__device__ __forceinline__ float xrow_max(float x) {
  auto s = __builtin_amdgcn_permlane16_swap(__float_as_uint(x), __float_as_uint(x), false, false);
  x = fmaxf(__uint_as_float(s[0]), __uint_as_float(s[1]));
  auto t = __builtin_amdgcn_permlane32_swap(__float_as_uint(x), __float_as_uint(x), false, false);
  return fmaxf(__uint_as_float(t[0]), __uint_as_float(t[1]));
}
__device__ __forceinline__ unsigned short f2bf(float f) { return __builtin_bit_cast(unsigned short, (__bf16)f); }
__device__ __forceinline__ float bf2f(unsigned short h) { return __uint_as_float((unsigned)h << 16); }
__device__ __forceinline__ unsigned pack_bf(float lo, float hi) { return (unsigned)f2bf(lo) | ((unsigned)f2bf(hi) << 16); }
__device__ __forceinline__ float rbf(float f) { return bf2f(f2bf(f)); }
__device__ __forceinline__ float score_of(float d) { return rbf(rbf(d) * 0.088388347648318440f) * 1.4426950408889634f; }
__device__ __forceinline__ unsigned f2p(float f) {
  const unsigned u = __float_as_uint(f);
  const bool fin = (u & 0x7f800000u) != 0x7f800000u;
  return (fin ? u + 0x80u : (f != f ? u | 0x00400000u : u)) >> 8;
}
__device__ __forceinline__ float p2f(u64 g) { return __uint_as_float((unsigned)g << 8); }
__device__ __forceinline__ bool tag_ok(u64 g, u64 tag) { return ((g ^ tag) >> 24) == 0; }

__device__ __forceinline__ bf16x8 col_frag(const u32x4 (&W)[8], int e) {
  const unsigned sel = (e & 1) ? 0x07060302u : 0x05040100u;
  u32x4 f;
#pragma unroll
  for (int q = 0; q < 4; ++q) f[q] = __builtin_amdgcn_perm(W[2 * q + 1][e >> 1], W[2 * q][e >> 1], sel);
  return __builtin_bit_cast(bf16x8, f);
}
__device__ __forceinline__ f32x4 mfma(u32x4 a, u32x4 b, f32x4 c) {
  return __builtin_amdgcn_mfma_f32_16x16x32_bf16(__builtin_bit_cast(bf16x8, a), __builtin_bit_cast(bf16x8, b), c, 0, 0, 0);
}
__device__ __forceinline__ void wait_until(u64 org, int due) {
#pragma unroll 1
  for (int n = 0; n < 2000; ++n) {
    const int rem = due - (int)(wall_clock64() - org);
    if (rem <= 0) break;
    if (rem > 400) __builtin_amdgcn_s_sleep(64);
    else if (rem > 50) __builtin_amdgcn_s_sleep(8);
    else __builtin_amdgcn_s_sleep(1);
  }
}
__device__ __forceinline__ constexpr int slot_due(int slot) { return (slot + PACE_OFF) * PACE_TAU / 100; }

struct Args {
  const char* x; const char* gw;
  const char* kA; const char* vA; const char* wqA; const char* woA;
  const char* kB; const char* vB; const char* wqB; const char* woB;
  u64* ws; unsigned short* out;
};

__global__ void __launch_bounds__(NTHR) k_fused(Args a) {
  __shared__ __attribute__((aligned(16))) float lds[L_TOT];
  const u64 t_start = wall_clock64();
  const int tid = threadIdx.x, lane = tid & 63;
  const int wave = __builtin_amdgcn_readfirstlane(tid >> 6);
  const int r = lane >> 4, c = lane & 15;
  const int idx = blockIdx.x >> 3, h = (blockIdx.x & 7) * 4 + (idx >> 3), role = (idx >> 2) & 1, b = idx & 3;
  gu64* ws = (gu64*)a.ws;
  unsigned* cnt = (unsigned*)(lds + L_CNT);
  unsigned short* qs = (unsigned short*)(lds + L_QS);
  const unsigned lane16 = lane * 16;

  if (tid < 8) cnt[tid] = 0u;
  asm volatile("s_waitcnt lgkmcnt(0)" ::: "memory");
  __builtin_amdgcn_s_barrier();
  __builtin_amdgcn_sched_barrier(0);

  u32x4 A[8], Bf[8];
  const u64 rowoff = (u64)(b * NH + h) * SK * 256;
  const int i0 = wave * 5, nA = role ? 128 - NA_OWN : NA_OWN;
  const u64 kA = (u64)a.kA + rowoff + (u64)(role ? NA_OWN : 0) * 8192, vA = (u64)a.vA + rowoff + (u64)(role ? NA_OWN : 0) * 8192;
  const u64 kB = (u64)a.kB + rowoff + (u64)(SK - WINB) * 256, vB = (u64)a.vB + rowoff + (u64)(SK - WINB) * 256;
#define KPTR(I) ((I) < nA ? kA + (u64)(I) * 8192 : kB + (u64)((I) - nA) * 8192)
#define VPTR(I) ((I) < nA ? vA + (u64)(I) * 8192 : vB + (u64)((I) - nA) * 8192)
#define ISSUE(KP, VP, BUF)                                                                     \
  {                                                                                            \
    const u64 kp_ = (KP), vp_ = (VP);                                                          \
    _Pragma("unroll") for (int i_ = 0; i_ < 4; ++i_) BUF[i_] = ld_nt(kp_, lane16 + i_ * 1024);     \
    _Pragma("unroll") for (int i_ = 0; i_ < 4; ++i_) BUF[4 + i_] = ld_nt(vp_, lane16 + i_ * 1024); \
    __builtin_amdgcn_sched_barrier(0);                                                         \
  }
  const u64 k0 = KPTR(i0), v0 = VPTR(i0);
  if (__builtin_expect(wave >= 4, 1)) {
    ISSUE(k0, v0, A)
    ISSUE(k0 + 4096, v0 + 4096, Bf)
  } else {
    const u64 ep0 = __hip_atomic_load(ws + W_EPOCH, RLX_AGENT);
    const u32x4 xa = ld_w(a.x + ((c & 3) * DM + wave * 128 + (c >> 2) * 32 + r * 8) * 2);
    const char* wq = (role ? a.wqB : a.wqA) + ((size_t)(wave * 128 + (c >> 2) * 32 + r * 8) * HD + h * DH + b * 32 + (c & 3) * 8) * 2;
#pragma unroll
    for (int j = 0; j < 8; ++j) Bf[j] = ld_w(wq + (size_t)j * HD * 2);
    __builtin_amdgcn_sched_barrier(0);
    ISSUE(k0, v0, A)
    const u64 tag = (ep0 + 1ull) << 24;
    if (blockIdx.x < 8 && wave == 1 && lane == 0) __hip_atomic_store(ws + W_T0 + blockIdx.x, tag | (t_start & 0xffffffull), RLX_AGENT);
    __builtin_amdgcn_sched_barrier(0);
#pragma unroll
    for (int hf = 0; hf < 2; ++hf) {
      f32x4 d_[4];
#pragma unroll
      for (int e = 0; e < 4; ++e) d_[e] = mfma(xa, __builtin_bit_cast(u32x4, col_frag(Bf, hf * 4 + e)), f32x4{0.f, 0.f, 0.f, 0.f});
      if ((c >> 2) == r) {
#pragma unroll
        for (int bb = 0; bb < 4; ++bb)
          *(f32x4*)(lds + L_QP + ((wave * 4 + r) * 4 + bb) * 32 + (c & 3) * 8 + hf * 4) = f32x4{d_[0][bb], d_[1][bb], d_[2][bb], d_[3][bb]};
      }
    }
    asm volatile("s_waitcnt lgkmcnt(0)" ::: "memory");
    if (lane == 0) __hip_atomic_fetch_add(cnt, 1u, RLX_WG);
    while (__hip_atomic_load(cnt, RLX_WG) < 4u) __builtin_amdgcn_s_sleep(1);
    asm volatile("" ::: "memory");
    float qv = 0.f;
#pragma unroll
    for (int p = 0; p < 8; ++p) qv += lds[L_QP + (((lane >> 5) * 8 + p) * 4 + wave) * 32 + (lane & 31)];
    qv = x32_sum(qv);
    if (lane < 32) __hip_atomic_store(ws + W_QG + ((h * 2 + role) * 4 + wave) * 128 + b * 32 + lane, tag | (u64)f2bf(qv), RLX_AGENT);
    if (wave == 0) {
      u64 g_[4], g0 = 0;
      for (unsigned spins = 0; spins < SPIN_CAP; ++spins) {
        bool ok = true;
        g0 = __hip_atomic_load(ws + W_T0 + (lane & 7), RLX_AGENT);
#pragma unroll
        for (int k = 0; k < 4; ++k) {
          g_[k] = __hip_atomic_load(ws + W_QG + ((h * 2 + (k >> 1)) * 4 + b) * 128 + (k & 1) * 64 + lane, RLX_AGENT);
          ok &= tag_ok(g_[k], tag);
        }
        if (__all(ok)) break;
        __builtin_amdgcn_s_sleep(2);
      }
#pragma unroll
      for (int k = 0; k < 4; ++k) qs[k * 64 + lane] = (unsigned short)g_[k];
      const int d = (int)(((unsigned)t_start - (unsigned)g0) << 8) >> 8;
      int back = (tag_ok(g0, tag) && d < 1000 && d > -1000) ? d : -100000;
      back = max(back, __shfl_xor(back, 1));
      back = max(back, __shfl_xor(back, 2));
      back = max(back, __shfl_xor(back, 4));
      if (back == -100000) back = 0;
      if (lane == 0) { *(u64*)(cnt + 4) = ep0; *(u64*)(cnt + 8) = t_start - (long long)back; }
      asm volatile("s_waitcnt lgkmcnt(0)" ::: "memory");
      if (lane == 0) __hip_atomic_store(cnt + 1, 1u, RLX_WG);
    }
    __builtin_amdgcn_sched_barrier(0);
    ISSUE(k0 + 4096, v0 + 4096, Bf)
  }
  while (__hip_atomic_load(cnt + 1, RLX_WG) == 0u) __builtin_amdgcn_s_sleep(2);
  asm volatile("" ::: "memory");
  const u64 epoch = ((u64)(unsigned)__builtin_amdgcn_readfirstlane(cnt[5]) << 32) | (unsigned)__builtin_amdgcn_readfirstlane(cnt[4]);
  const u64 tag = (epoch + 1ull) << 24;
  const u64 t_org = ((u64)(unsigned)__builtin_amdgcn_readfirstlane(cnt[9]) << 32) | (unsigned)__builtin_amdgcn_readfirstlane(cnt[8]);
  u32x4 q = *(const u32x4*)(qs + (i0 < nA ? 0 : 128) + c * 8);

  float m = -INFINITY, l = 0.f;
  f32x2 acc[4];
#pragma unroll
  for (int k = 0; k < 4; ++k) acc[k] = f32x2{0.f, 0.f};

#define ROWMERGE()                                                                             \
    const float L_ = xrow_sum(l);                                                              \
    const float o0_ = tr16_sum(tr32_sum(acc[0][0], acc[2][0]), tr32_sum(acc[1][0], acc[3][0])); \
    const float o1_ = tr16_sum(tr32_sum(acc[0][1], acc[2][1]), tr32_sum(acc[1][1], acc[3][1]));
#define FLUSH_EXT()                                                                            \
  {                                                                                            \
    ROWMERGE()                                                                                 \
    *(f32x2*)(lds + L_EO + wave * 128 + c * 8 + r * 2) = f32x2{o0_, o1_};                      \
    if (lane == 0) { lds[L_EM + wave] = m; lds[L_EL + wave] = L_; }                            \
    m = -INFINITY; l = 0.f;                                                                    \
    _Pragma("unroll") for (int k = 0; k < 4; ++k) acc[k] = f32x2{0.f, 0.f};                    \
    asm volatile("s_waitcnt lgkmcnt(0)" ::: "memory");                                         \
    if (lane == 0) __hip_atomic_fetch_add(cnt + 2, 1u, RLX_WG);                                \
  }
#define COMPUTE(BUF)                                                                           \
  {                                                                                            \
    float t_[4];                                                                               \
    _Pragma("unroll") for (int i_ = 0; i_ < 4; ++i_) {                                         \
      float d_ = dot2(BUF[i_][0], q[0], 0.f);                                                  \
      d_ = dot2(BUF[i_][1], q[1], d_);                                                         \
      d_ = dot2(BUF[i_][2], q[2], d_);                                                         \
      d_ = dot2(BUF[i_][3], q[3], d_);                                                         \
      t_[i_] = score_of(row16_sum(d_));                                                        \
    }                                                                                          \
    const float mn_ = fmaxf(m, xrow_max(fmaxf(fmaxf(t_[0], t_[1]), fmaxf(t_[2], t_[3]))));     \
    const float ms_ = (mn_ == -INFINITY) ? 0.f : mn_;                                          \
    const float al_ = __builtin_amdgcn_exp2f(m - ms_);                                         \
    m = mn_;                                                                                   \
    l *= al_;                                                                                  \
    _Pragma("unroll") for (int k = 0; k < 4; ++k) acc[k] *= al_;                               \
    _Pragma("unroll") for (int i_ = 0; i_ < 4; ++i_) {                                         \
      const float p_ = __builtin_amdgcn_exp2f(t_[i_] - ms_);                                   \
      l += p_;                                                                                 \
      _Pragma("unroll") for (int k = 0; k < 4; ++k) {                                          \
        const unsigned w_ = BUF[4 + i_][k];                                                    \
        const f32x2 v2_ = {__uint_as_float(w_ << 16), __uint_as_float(w_ & 0xffff0000u)};      \
        acc[k] = __builtin_elementwise_fma(f32x2{p_, p_}, v2_, acc[k]);                        \
      }                                                                                        \
    }                                                                                          \
    _Pragma("unroll") for (int k = 0; k < 4; ++k) asm volatile("" : "+v"(acc[k]));   \
    __builtin_amdgcn_sched_barrier(0);                                                         \
  }
#define PACE(K)                                                                                \
  {                                                                                            \
    wait_until(t_org, slot_due(32 + (wave >> 2) * 32 + (K) * 4 + (wave & 3)));                 \
    __builtin_amdgcn_sched_barrier(0);                                                         \
  }
#pragma unroll 1
  for (int it = 1; it <= 4; ++it) {
    const u64 kn = KPTR(i0 + it), vn = VPTR(i0 + it);
    COMPUTE(A)
    PACE(2 * it - 2)
    ISSUE(kn, vn, A)
    COMPUTE(Bf)
    PACE(2 * it - 1)
    ISSUE(kn + 4096, vn + 4096, Bf)
    if (i0 + it == nA) {
      FLUSH_EXT()
      q = *(const u32x4*)(qs + 128 + c * 8);
    }
  }
  COMPUTE(A)
  COMPUTE(Bf)
  if (lane == 0) __hip_atomic_fetch_add(cnt + 6, 1u, RLX_WG);
  if (role && i0 + 5 <= nA) FLUSH_EXT()

  if (wave < 8) {
    const int cb = wave & 3, rp = wave >> 2;
    const char* wo = (role ? a.woB : a.woA) + ((size_t)(h * DH + rp * 64 + r * 8) * DM + cb * 128 + c * 8) * 2;
#pragma unroll 1
    for (int hf = 0; hf < 2; ++hf) {
      wait_until(t_org, slot_due(68 + (wave * 2 + hf) * 4));
#pragma unroll
      for (int j = 0; j < 8; ++j) A[j] = ld_w(wo + (hf * 32 + j) * DM * 2);
#pragma unroll
      for (int e = 0; e < 8; ++e) *(bf16x8*)(lds + L_WF + (((cb * 4 + rp * 2 + hf) * 8 + e) * 64 + lane) * 4) = col_frag(A, e);
    }
    asm volatile("s_waitcnt lgkmcnt(0)" ::: "memory");
    if (lane == 0) __hip_atomic_fetch_add(cnt + 3, 1u, RLX_WG);
  } else if (wave == 8) {
#pragma unroll
    for (int j = 0; j < 4; ++j) A[j] = ld_w(a.x + j * DM * 2 + lane16);
    A[4] = ld_w(a.gw + lane16);
#pragma unroll
    for (int bb = 0; bb < 4; ++bb) {
      float z = 0.f;
#pragma unroll
      for (int k = 0; k < 4; ++k) {
        z += rbf(__uint_as_float(A[bb][k] << 16) * __uint_as_float(A[4][k] << 16));
        z += rbf(__uint_as_float(A[bb][k] & 0xffff0000u) * __uint_as_float(A[4][k] & 0xffff0000u));
      }
      z = rbf(xrow_sum(row16_sum(z)));
      if (lane == 0) lds[L_G + bb] = rbf(1.f / (1.f + __expf(-z)));
    }
  }
  __builtin_amdgcn_sched_barrier(0);
  if (role && wave == 0) {
    while (__hip_atomic_load(cnt + 2, RLX_WG) < 10u) __builtin_amdgcn_s_sleep(8);
    asm volatile("" ::: "memory");
    float Mx = -INFINITY;
#pragma unroll
    for (int w = 0; w < 10; ++w) Mx = fmaxf(Mx, lds[L_EM + w]);
    const unsigned Mp = f2p(Mx);
    const float Mr = __uint_as_float(Mp << 8);
    float e0_ = 0.f, e1_ = 0.f, Ls = 0.f;
#pragma unroll
    for (int w = 0; w < 10; ++w) {
      const float mw_ = lds[L_EM + w];
      const float w_ = (mw_ == -INFINITY) ? 0.f : __builtin_amdgcn_exp2f(mw_ - Mr);
      e0_ = fmaf(w_, lds[L_EO + w * 128 + lane], e0_);
      e1_ = fmaf(w_, lds[L_EO + w * 128 + 64 + lane], e1_);
      Ls = fmaf(w_, lds[L_EL + w], Ls);
    }
    gu64* og = ws + W_OG + (h * 4 + b) * 144;
    __hip_atomic_store(og + lane, tag | (u64)f2p(e0_), RLX_AGENT);
    __hip_atomic_store(og + 64 + lane, tag | (u64)f2p(e1_), RLX_AGENT);
    if (lane < 2) __hip_atomic_store(og + 128 + lane, tag | (u64)(lane ? f2p(Ls) : Mp), RLX_AGENT);
  }
  if (!role && wave == 0) {
    const gu64* og = ws + W_OG + (h * 4 + b) * 144;
    u64 g_[8] = {0, 0, 0, 0, 0, 0, 0, 0}, gm = 0, gl = 0;
    while (__hip_atomic_load(cnt + 6, RLX_WG) < 12u) __builtin_amdgcn_s_sleep(16);
    for (unsigned spins = 0; spins < SPIN_CAP; ++spins) {
      gm = __hip_atomic_load(og + 128, RLX_AGENT);
      gl = __hip_atomic_load(og + 129, RLX_AGENT);
      if (__all(tag_ok(gm, tag) && tag_ok(gl, tag))) {
        bool ok = true;
#pragma unroll
        for (int k = 0; k < 8; ++k) {
          g_[k] = __hip_atomic_load(og + c * 8 + k, RLX_AGENT);
          ok &= tag_ok(g_[k], tag);
        }
        if (__all(ok)) break;
      }
      __builtin_amdgcn_s_sleep(16);
    }
    const float mh = p2f(gm), mn = fmaxf(m, mh), ms = (mn == -INFINITY) ? 0.f : mn;
    const float wo_ = __builtin_amdgcn_exp2f(m - ms), wh = r == 0 ? __builtin_amdgcn_exp2f(mh - ms) : 0.f;
    m = mn;
    l = fmaf(wh, p2f(gl), l * wo_);
#pragma unroll
    for (int k = 0; k < 8; ++k) acc[k >> 1][k & 1] = fmaf(wh, p2f(g_[k]), acc[k >> 1][k & 1] * wo_);
  }
  {
    ROWMERGE()
    *(unsigned*)(lds + L_FO + wave * 68 + c * 4 + r) = pack_bf(o0_, o1_);
    if (lane == 0) { lds[L_FM + wave] = m; lds[L_FL + wave] = L_; }
  }

  if (wave >= 8) {
    asm volatile("s_waitcnt lgkmcnt(0)" ::: "memory");
    __builtin_amdgcn_s_barrier();
  } else {
    const int cb = wave & 3, e0 = (wave >> 2) * 4;
    gu64* ydst = ws + W_YG + ((size_t)((cb * 8 + e0 + r) * 4 + b) * 64 + h * 2 + role) * 16 + c;
    while (__hip_atomic_load(cnt + 3, RLX_WG) < 8u) __builtin_amdgcn_s_sleep(4);
    asm volatile("" ::: "memory");
#pragma unroll
    for (int k = 0; k < 8; ++k) {
      A[k] = *(const u32x4*)(lds + L_WF + (((cb * 4 + (k >> 1)) * 8 + e0 + (k & 1)) * 64 + lane) * 4);
      Bf[k] = *(const u32x4*)(lds + L_WF + (((cb * 4 + (k >> 1)) * 8 + e0 + 2 + (k & 1)) * 64 + lane) * 4);
    }
    asm volatile("" : "+v"(ydst));
    asm volatile("s_waitcnt lgkmcnt(0)" ::: "memory");
    __builtin_amdgcn_s_barrier();
    asm volatile("" ::: "memory");
    u32x4 av[4];
#pragma unroll
    for (int kb = 0; kb < 4; ++kb) av[kb] = *(const u32x4*)(lds + L_FO + c * 68 + kb * 16 + r * 4);
    const f32x4 mw = *(const f32x4*)(lds + L_FM + r * 4), lw = *(const f32x4*)(lds + L_FL + r * 4);
    f32x4 d[4];
#pragma unroll
    for (int e = 0; e < 4; ++e) d[e] = f32x4{0.f, 0.f, 0.f, 0.f};
#pragma unroll
    for (int kb = 0; kb < 4; ++kb) {
      d[0] = mfma(av[kb], A[kb * 2], d[0]);
      d[1] = mfma(av[kb], A[kb * 2 + 1], d[1]);
      d[2] = mfma(av[kb], Bf[kb * 2], d[2]);
      d[3] = mfma(av[kb], Bf[kb * 2 + 1], d[3]);
    }
    const float M = xrow_max(fmaxf(fmaxf(mw[0], mw[1]), fmaxf(mw[2], mw[3])));
    float sw[4];
#pragma unroll
    for (int k = 0; k < 4; ++k) sw[k] = (mw[k] == -INFINITY) ? 0.f : __builtin_amdgcn_exp2f(mw[k] - M);
    const float L = xrow_sum((sw[0] * lw[0] + sw[1] * lw[1]) + (sw[2] * lw[2] + sw[3] * lw[3]));
    float ye[4];
#pragma unroll
    for (int e = 0; e < 4; ++e) ye[e] = (sw[0] * d[e][0] + sw[1] * d[e][1]) + (sw[2] * d[e][2] + sw[3] * d[e][3]);
    const float y = tr16_sum(tr32_sum(ye[0], ye[2]), tr32_sum(ye[1], ye[3])) * __builtin_amdgcn_rcpf(L);
    __hip_atomic_store(ydst, tag | (u64)f2p(y), RLX_AGENT);
  }
  __builtin_amdgcn_s_barrier();

  if ((blockIdx.x & 1) == 0) {
    const int rr = blockIdx.x >> 1;
    wait_until(t_org, slot_due(159) + FIN_DELAY);
    u64 g_ = 0;
    for (unsigned spins = 0; spins < SPIN_CAP; ++spins) {
      g_ = __hip_atomic_load(ws + W_YG + (size_t)rr * 1024 + tid, RLX_AGENT);
      if (__all(tag_ok(g_, tag))) break;
      __builtin_amdgcn_s_sleep(1);
    }
    const float v = x32_sum(p2f(g_));
    if (lane < 32) lds[L_QP + lane * 20 + wave] = v;
    __syncthreads();
    if (wave == 0) {
      const float* src = lds + L_QP + (lane & 31) * 20 + (lane >> 5) * 8;
      const f32x4 u = *(const f32x4*)src + *(const f32x4*)(src + 4);
      float t = x32_sum((u[0] + u[1]) + (u[2] + u[3]));
      const float g = lds[L_G + (rr & 3)];
      t = rbf(rbf(t) * ((lane & 16) ? rbf(1.f - g) : g));
      t = x16_sum(t);
      if (lane < 16) a.out[(rr & 3) * DM + (rr >> 5) * 128 + c * 8 + ((rr >> 2) & 7)] = f2bf(t);
      if (blockIdx.x == 0 && lane == 0) __hip_atomic_store(ws + W_EPOCH, epoch + 1ull, RLX_AGENT);
    }
  }
}

}

extern "C" void kernel_launch(void* const* d_in, const int* in_sizes, int n_in, void* d_out, int out_size, void* d_ws,
                              size_t ws_size, hipStream_t stream) {
  (void)in_sizes; (void)n_in; (void)out_size; (void)ws_size;
  Args a;
  a.x = (const char*)d_in[0]; a.gw = (const char*)d_in[10];
  a.kA = (const char*)d_in[1]; a.vA = (const char*)d_in[2]; a.wqA = (const char*)d_in[3]; a.woA = (const char*)d_in[4];
  a.kB = (const char*)d_in[6]; a.vB = (const char*)d_in[7]; a.wqB = (const char*)d_in[8]; a.woB = (const char*)d_in[9];
  a.ws = (u64*)d_ws; a.out = (unsigned short*)d_out;
  hipLaunchKernelGGL(k_fused, dim3(NWG), dim3(NTHR), 0, stream, a);
}
```

```cpp
#include <hip/hip_runtime.h>
#include <hip/hip_bf16.h>
#include <stdint.h>

typedef unsigned u32x4 __attribute__((ext_vector_type(4)));
typedef float f32x4 __attribute__((ext_vector_type(4)));
typedef float f32x2 __attribute__((ext_vector_type(2)));
typedef short bf16x8 __attribute__((ext_vector_type(8)));
typedef __bf16 bf16x2_t __attribute__((ext_vector_type(2)));
typedef unsigned long long u64;
#define GAS __attribute__((address_space(1)))
typedef const GAS u32x4* gptr4;
typedef const GAS char* gchar;
typedef GAS u64 gu64;
#define RLX_AGENT __ATOMIC_RELAXED, __HIP_MEMORY_SCOPE_AGENT
#define RLX_WG __ATOMIC_RELAXED, __HIP_MEMORY_SCOPE_WORKGROUP

#define PACE_TAU 3060
#define PACE_OFF 7
#define FIN_DELAY 205

namespace {

constexpr int DM = 512, NH = 32, SK = 4096, DH = 128, WINB = 1024;
constexpr int HD = NH * DH;
constexpr int NWG = 256, NTHR = 1024;
constexpr int NA_OWN = 80;
constexpr unsigned SPIN_CAP = 1u << 17;

constexpr size_t W_EPOCH = 0;
constexpr size_t W_T0 = 32;
constexpr size_t W_QG = 64;
constexpr size_t W_OG = W_QG + 32 * 2 * 4 * 128;
constexpr size_t W_YG = W_OG + 32 * 4 * 144;

constexpr int L_FO = 0;
constexpr int L_FM = L_FO + 16 * 68;
constexpr int L_FL = L_FM + 16;
constexpr int L_EO = L_FL + 16;
constexpr int L_EM = L_EO + 1280;
constexpr int L_EL = L_EM + 16;
constexpr int L_QP = L_EL + 16;
constexpr int L_QS = L_QP + 2048;
constexpr int L_G = L_QS + 128;
constexpr int L_CNT = L_G + 4;
constexpr int L_WF = L_CNT + 12;
constexpr int L_TOT = L_WF + 32768;
static_assert(L_WF % 4 == 0 && L_CNT % 2 == 0 && L_QP % 4 == 0 && L_FM % 4 == 0 && L_QS % 4 == 0, "alignment");

__device__ __forceinline__ u32x4 ld_nt(u64 sbase, unsigned voff) { return __builtin_nontemporal_load((gptr4)((gchar)sbase + voff)); }
__device__ __forceinline__ u32x4 ld_w(const char* p) { return *(const u32x4*)p; }
__device__ __forceinline__ float dot2(unsigned k, unsigned q, float acc) {
  return __builtin_amdgcn_fdot2_f32_bf16(__builtin_bit_cast(bf16x2_t, k), __builtin_bit_cast(bf16x2_t, q), acc, false);
}
template <int CTRL> __device__ __forceinline__ float dpp(float x) {
  return __builtin_bit_cast(float, __builtin_amdgcn_mov_dpp(__builtin_bit_cast(int, x), CTRL, 0xf, 0xf, true));
}
__device__ __forceinline__ float row16_sum(float x) {
  x += dpp<0xB1>(x);
  x += dpp<0x4E>(x);
  x += dpp<0x141>(x);
  x += dpp<0x140>(x);
  return x;
}
__device__ __forceinline__ float tr32_sum(float a, float b) {
  auto t = __builtin_amdgcn_permlane32_swap(__float_as_uint(a), __float_as_uint(b), false, false);
  return __uint_as_float(t[0]) + __uint_as_float(t[1]);
}
__device__ __forceinline__ float tr16_sum(float a, float b) {
  auto t = __builtin_amdgcn_permlane16_swap(__float_as_uint(a), __float_as_uint(b), false, false);
  return __uint_as_float(t[0]) + __uint_as_float(t[1]);
}
__device__ __forceinline__ float x16_sum(float x) { return tr16_sum(x, x); }
__device__ __forceinline__ float x32_sum(float x) { return tr32_sum(x, x); }
__device__ __forceinline__ float xrow_sum(float x) { return x32_sum(x16_sum(x)); }
__device__ __forceinline__ float xrow_max(float x) {
  auto s = __builtin_amdgcn_permlane16_swap(__float_as_uint(x), __float_as_uint(x), false, false);
  x = fmaxf(__uint_as_float(s[0]), __uint_as_float(s[1]));
  auto t = __builtin_amdgcn_permlane32_swap(__float_as_uint(x), __float_as_uint(x), false, false);
  return fmaxf(__uint_as_float(t[0]), __uint_as_float(t[1]));
}
__device__ __forceinline__ unsigned short f2bf(float f) { return __builtin_bit_cast(unsigned short, (__bf16)f); }
__device__ __forceinline__ float bf2f(unsigned short h) { return __uint_as_float((unsigned)h << 16); }
__device__ __forceinline__ unsigned pack_bf(float lo, float hi) { return (unsigned)f2bf(lo) | ((unsigned)f2bf(hi) << 16); }
__device__ __forceinline__ float rbf(float f) { return bf2f(f2bf(f)); }
__device__ __forceinline__ float score_of(float d) { return rbf(rbf(d) * 0.088388347648318440f) * 1.4426950408889634f; }
__device__ __forceinline__ unsigned f2p(float f) {
  const unsigned u = __float_as_uint(f);
  const bool fin = (u & 0x7f800000u) != 0x7f800000u;
  return (fin ? u + 0x80u : (f != f ? u | 0x00400000u : u)) >> 8;
}
__device__ __forceinline__ float p2f(u64 g) { return __uint_as_float((unsigned)g << 8); }
__device__ __forceinline__ bool tag_ok(u64 g, u64 tag) { return ((g ^ tag) >> 24) == 0; }

__device__ __forceinline__ bf16x8 col_frag(const u32x4 (&W)[8], int e) {
  const unsigned sel = (e & 1) ? 0x07060302u : 0x05040100u;
  u32x4 f;
#pragma unroll
  for (int q = 0; q < 4; ++q) f[q] = __builtin_amdgcn_perm(W[2 * q + 1][e >> 1], W[2 * q][e >> 1], sel);
  return __builtin_bit_cast(bf16x8, f);
}
__device__ __forceinline__ f32x4 mfma(u32x4 a, u32x4 b, f32x4 c) {
  return __builtin_amdgcn_mfma_f32_16x16x32_bf16(__builtin_bit_cast(bf16x8, a), __builtin_bit_cast(bf16x8, b), c, 0, 0, 0);
}
__device__ __forceinline__ void wait_until(u64 org, int due) {
#pragma unroll 1
  for (int n = 0; n < 2000; ++n) {
    const int rem = due - (int)(wall_clock64() - org);
    if (rem <= 0) break;
    if (rem > 400) __builtin_amdgcn_s_sleep(64);
    else if (rem > 50) __builtin_amdgcn_s_sleep(8);
    else __builtin_amdgcn_s_sleep(1);
  }
}
__device__ __forceinline__ constexpr int slot_due(int slot) { return (slot + PACE_OFF) * PACE_TAU / 100; }

struct Args {
  const char* x; const char* gw;
  const char* kA; const char* vA; const char* wqA; const char* woA;
  const char* kB; const char* vB; const char* wqB; const char* woB;
  u64* ws; unsigned short* out;
};

__global__ void __launch_bounds__(NTHR) k_fused(Args a) {
  __shared__ __attribute__((aligned(16))) float lds[L_TOT];
  const u64 t_start = wall_clock64();
  const int tid = threadIdx.x, lane = tid & 63;
  const int wave = __builtin_amdgcn_readfirstlane(tid >> 6);
  const int r = lane >> 4, c = lane & 15;
  const int idx = blockIdx.x >> 3, h = (blockIdx.x & 7) * 4 + (idx >> 3), role = (idx >> 2) & 1, b = idx & 3;
  gu64* ws = (gu64*)a.ws;
  unsigned* cnt = (unsigned*)(lds + L_CNT);
  unsigned short* qs = (unsigned short*)(lds + L_QS);
  const unsigned lane16 = lane * 16;

  if (tid < 8) cnt[tid] = 0u;
  asm volatile("s_waitcnt lgkmcnt(0)" ::: "memory");
  __builtin_amdgcn_s_barrier();
  __builtin_amdgcn_sched_barrier(0);

  u32x4 A[8], Bf[8];
  const u64 rowoff = (u64)(b * NH + h) * SK * 256;
  const int i0 = wave * 5, nA = role ? 128 - NA_OWN : NA_OWN;
  const u64 kA = (u64)a.kA + rowoff + (u64)(role ? NA_OWN : 0) * 8192, vA = (u64)a.vA + rowoff + (u64)(role ? NA_OWN : 0) * 8192;
  const u64 kB = (u64)a.kB + rowoff + (u64)(SK - WINB) * 256, vB = (u64)a.vB + rowoff + (u64)(SK - WINB) * 256;
#define KPTR(I) ((I) < nA ? kA + (u64)(I) * 8192 : kB + (u64)((I) - nA) * 8192)
#define VPTR(I) ((I) < nA ? vA + (u64)(I) * 8192 : vB + (u64)((I) - nA) * 8192)
#define ISSUE(KP, VP, BUF)                                                                     \
  {                                                                                            \
    const u64 kp_ = (KP), vp_ = (VP);                                                          \
    _Pragma("unroll") for (int i_ = 0; i_ < 4; ++i_) BUF[i_] = ld_nt(kp_, lane16 + i_ * 1024);     \
    _Pragma("unroll") for (int i_ = 0; i_ < 4; ++i_) BUF[4 + i_] = ld_nt(vp_, lane16 + i_ * 1024); \
    __builtin_amdgcn_sched_barrier(0);                                                         \
  }
  const u64 k0 = KPTR(i0), v0 = VPTR(i0);
  ISSUE(k0, v0, A)
  if (__builtin_expect(wave < 4, 0)) {
    const u32x4 xa = ld_w(a.x + ((c & 3) * DM + wave * 128 + (c >> 2) * 32 + r * 8) * 2);
    const char* wq = (role ? a.wqB : a.wqA) + ((size_t)(wave * 128 + (c >> 2) * 32 + r * 8) * HD + h * DH + b * 32 + (c & 3) * 8) * 2;
#pragma unroll
    for (int j = 0; j < 8; ++j) Bf[j] = ld_w(wq + (size_t)j * HD * 2);
    __builtin_amdgcn_sched_barrier(0);
    u64 ep0;
    asm volatile("s_load_dwordx2 %0, %1, 0x0\n\ts_waitcnt lgkmcnt(0)" : "=&s"(ep0) : "s"(a.ws) : "memory");
    const u64 tag = (ep0 + 1ull) << 24;
    if (blockIdx.x < 8 && wave == 1 && lane == 0) __hip_atomic_store(ws + W_T0 + blockIdx.x, tag | (t_start & 0xffffffull), RLX_AGENT);
    __builtin_amdgcn_sched_barrier(0);
#pragma unroll
    for (int hf = 0; hf < 2; ++hf) {
      f32x4 d_[4];
#pragma unroll
      for (int e = 0; e < 4; ++e) d_[e] = mfma(xa, __builtin_bit_cast(u32x4, col_frag(Bf, hf * 4 + e)), f32x4{0.f, 0.f, 0.f, 0.f});
      if ((c >> 2) == r) {
#pragma unroll
        for (int bb = 0; bb < 4; ++bb)
          *(f32x4*)(lds + L_QP + ((wave * 4 + r) * 4 + bb) * 32 + (c & 3) * 8 + hf * 4) = f32x4{d_[0][bb], d_[1][bb], d_[2][bb], d_[3][bb]};
      }
    }
    asm volatile("s_waitcnt lgkmcnt(0)" ::: "memory");
    if (lane == 0) __hip_atomic_fetch_add(cnt, 1u, RLX_WG);
    while (__hip_atomic_load(cnt, RLX_WG) < 4u) __builtin_amdgcn_s_sleep(1);
    asm volatile("" ::: "memory");
    float qv = 0.f;
#pragma unroll
    for (int p = 0; p < 8; ++p) qv += lds[L_QP + (((lane >> 5) * 8 + p) * 4 + wave) * 32 + (lane & 31)];
    qv = x32_sum(qv);
    if (lane < 32) __hip_atomic_store(ws + W_QG + ((h * 2 + role) * 4 + wave) * 128 + b * 32 + lane, tag | (u64)f2bf(qv), RLX_AGENT);
    if (wave == 0) {
      u64 g_[4], g0 = 0;
      for (unsigned spins = 0; spins < SPIN_CAP; ++spins) {
        bool ok = true;
        g0 = __hip_atomic_load(ws + W_T0 + (lane & 7), RLX_AGENT);
#pragma unroll
        for (int k = 0; k < 4; ++k) {
          g_[k] = __hip_atomic_load(ws + W_QG + ((h * 2 + (k >> 1)) * 4 + b) * 128 + (k & 1) * 64 + lane, RLX_AGENT);
          ok &= tag_ok(g_[k], tag);
        }
        if (__all(ok)) break;
        __builtin_amdgcn_s_sleep(2);
      }
#pragma unroll
      for (int k = 0; k < 4; ++k) qs[k * 64 + lane] = (unsigned short)g_[k];
      const int d = (int)(((unsigned)t_start - (unsigned)g0) << 8) >> 8;
      int back = (tag_ok(g0, tag) && d < 1000 && d > -1000) ? d : -100000;
      back = max(back, __shfl_xor(back, 1));
      back = max(back, __shfl_xor(back, 2));
      back = max(back, __shfl_xor(back, 4));
      if (back == -100000) back = 0;
      if (lane == 0) { *(u64*)(cnt + 4) = ep0; *(u64*)(cnt + 8) = t_start - (long long)back; }
      asm volatile("s_waitcnt lgkmcnt(0)" ::: "memory");
      if (lane == 0) __hip_atomic_store(cnt + 1, 1u, RLX_WG);
    }
    __builtin_amdgcn_sched_barrier(0);
  }
  ISSUE(k0 + 4096, v0 + 4096, Bf)
  while (__hip_atomic_load(cnt + 1, RLX_WG) == 0u) __builtin_amdgcn_s_sleep(2);
  asm volatile("" ::: "memory");
  const u64 epoch = ((u64)(unsigned)__builtin_amdgcn_readfirstlane(cnt[5]) << 32) | (unsigned)__builtin_amdgcn_readfirstlane(cnt[4]);
  const u64 tag = (epoch + 1ull) << 24;
  const u64 t_org = ((u64)(unsigned)__builtin_amdgcn_readfirstlane(cnt[9]) << 32) | (unsigned)__builtin_amdgcn_readfirstlane(cnt[8]);
  u32x4 q = *(const u32x4*)(qs + (i0 < nA ? 0 : 128) + c * 8);

  float m = -INFINITY, l = 0.f;
  f32x2 acc[4];
#pragma unroll
  for (int k = 0; k < 4; ++k) acc[k] = f32x2{0.f, 0.f};

#define ROWMERGE()                                                                             \
    const float L_ = xrow_sum(l);                                                              \
    const float o0_ = tr16_sum(tr32_sum(acc[0][0], acc[2][0]), tr32_sum(acc[1][0], acc[3][0])); \
    const float o1_ = tr16_sum(tr32_sum(acc[0][1], acc[2][1]), tr32_sum(acc[1][1], acc[3][1]));
#define FLUSH_EXT()                                                                            \
  {                                                                                            \
    ROWMERGE()                                                                                 \
    *(f32x2*)(lds + L_EO + wave * 128 + c * 8 + r * 2) = f32x2{o0_, o1_};                      \
    if (lane == 0) { lds[L_EM + wave] = m; lds[L_EL + wave] = L_; }                            \
    m = -INFINITY; l = 0.f;                                                                    \
    _Pragma("unroll") for (int k = 0; k < 4; ++k) acc[k] = f32x2{0.f, 0.f};                    \
    asm volatile("s_waitcnt lgkmcnt(0)" ::: "memory");                                         \
    if (lane == 0) __hip_atomic_fetch_add(cnt + 2, 1u, RLX_WG);                                \
  }
#define COMPUTE(BUF)                                                                           \
  {                                                                                            \
    float t_[4];                                                                               \
    _Pragma("unroll") for (int i_ = 0; i_ < 4; ++i_) {                                         \
      float d_ = dot2(BUF[i_][0], q[0], 0.f);                                                  \
      d_ = dot2(BUF[i_][1], q[1], d_);                                                         \
      d_ = dot2(BUF[i_][2], q[2], d_);                                                         \
      d_ = dot2(BUF[i_][3], q[3], d_);                                                         \
      t_[i_] = score_of(row16_sum(d_));                                                        \
    }                                                                                          \
    const float mn_ = fmaxf(m, xrow_max(fmaxf(fmaxf(t_[0], t_[1]), fmaxf(t_[2], t_[3]))));     \
    const float ms_ = (mn_ == -INFINITY) ? 0.f : mn_;                                          \
    const float al_ = __builtin_amdgcn_exp2f(m - ms_);                                         \
    m = mn_;                                                                                   \
    l *= al_;                                                                                  \
    _Pragma("unroll") for (int k = 0; k < 4; ++k) acc[k] *= al_;                               \
    _Pragma("unroll") for (int i_ = 0; i_ < 4; ++i_) {                                         \
      const float p_ = __builtin_amdgcn_exp2f(t_[i_] - ms_);                                   \
      l += p_;                                                                                 \
      _Pragma("unroll") for (int k = 0; k < 4; ++k) {                                          \
        const unsigned w_ = BUF[4 + i_][k];                                                    \
        const f32x2 v2_ = {__uint_as_float(w_ << 16), __uint_as_float(w_ & 0xffff0000u)};      \
        acc[k] = __builtin_elementwise_fma(f32x2{p_, p_}, v2_, acc[k]);                        \
      }                                                                                        \
    }                                                                                          \
    _Pragma("unroll") for (int k = 0; k < 4; ++k) asm volatile("" : "+v"(acc[k]));   \
    __builtin_amdgcn_sched_barrier(0);                                                         \
  }
#define PACE(K)                                                                                \
  {                                                                                            \
    wait_until(t_org, slot_due(32 + (wave >> 2) * 32 + (K) * 4 + (wave & 3)));                 \
    __builtin_amdgcn_sched_barrier(0);                                                         \
  }
#pragma unroll 1
  for (int it = 1; it <= 4; ++it) {
    const u64 kn = KPTR(i0 + it), vn = VPTR(i0 + it);
    COMPUTE(A)
    PACE(2 * it - 2)
    ISSUE(kn, vn, A)
    COMPUTE(Bf)
    PACE(2 * it - 1)
    ISSUE(kn + 4096, vn + 4096, Bf)
    if (i0 + it == nA) {
      FLUSH_EXT()
      q = *(const u32x4*)(qs + 128 + c * 8);
    }
  }
  COMPUTE(A)
  COMPUTE(Bf)
  if (lane == 0) __hip_atomic_fetch_add(cnt + 6, 1u, RLX_WG);
  if (role && i0 + 5 <= nA) FLUSH_EXT()

  if (wave < 8) {
    const int cb = wave & 3, rp = wave >> 2;
    const char* wo = (role ? a.woB : a.woA) + ((size_t)(h * DH + rp * 64 + r * 8) * DM + cb * 128 + c * 8) * 2;
#pragma unroll 1
    for (int hf = 0; hf < 2; ++hf) {
      wait_until(t_org, slot_due(68 + (wave * 2 + hf) * 4));
#pragma unroll
      for (int j = 0; j < 8; ++j) A[j] = ld_w(wo + (hf * 32 + j) * DM * 2);
#pragma unroll
      for (int e = 0; e < 8; ++e) *(bf16x8*)(lds + L_WF + (((cb * 4 + rp * 2 + hf) * 8 + e) * 64 + lane) * 4) = col_frag(A, e);
    }
    asm volatile("s_waitcnt lgkmcnt(0)" ::: "memory");
    if (lane == 0) __hip_atomic_fetch_add(cnt + 3, 1u, RLX_WG);
  } else if (wave == 8) {
#pragma unroll
    for (int j = 0; j < 4; ++j) A[j] = ld_w(a.x + j * DM * 2 + lane16);
    A[4] = ld_w(a.gw + lane16);
#pragma unroll
    for (int bb = 0; bb < 4; ++bb) {
      float z = 0.f;
#pragma unroll
      for (int k = 0; k < 4; ++k) {
        z += rbf(__uint_as_float(A[bb][k] << 16) * __uint_as_float(A[4][k] << 16));
        z += rbf(__uint_as_float(A[bb][k] & 0xffff0000u) * __uint_as_float(A[4][k] & 0xffff0000u));
      }
      z = rbf(xrow_sum(row16_sum(z)));
      if (lane == 0) lds[L_G + bb] = rbf(1.f / (1.f + __expf(-z)));
    }
  }
  __builtin_amdgcn_sched_barrier(0);
  if (role && wave == 0) {
    while (__hip_atomic_load(cnt + 2, RLX_WG) < 10u) __builtin_amdgcn_s_sleep(8);
    asm volatile("" ::: "memory");
    float Mx = -INFINITY;
#pragma unroll
    for (int w = 0; w < 10; ++w) Mx = fmaxf(Mx, lds[L_EM + w]);
    const unsigned Mp = f2p(Mx);
    const float Mr = __uint_as_float(Mp << 8);
    float e0_ = 0.f, e1_ = 0.f, Ls = 0.f;
#pragma unroll
    for (int w = 0; w < 10; ++w) {
      const float mw_ = lds[L_EM + w];
      const float w_ = (mw_ == -INFINITY) ? 0.f : __builtin_amdgcn_exp2f(mw_ - Mr);
      e0_ = fmaf(w_, lds[L_EO + w * 128 + lane], e0_);
      e1_ = fmaf(w_, lds[L_EO + w * 128 + 64 + lane], e1_);
      Ls = fmaf(w_, lds[L_EL + w], Ls);
    }
    gu64* og = ws + W_OG + (h * 4 + b) * 144;
    __hip_atomic_store(og + lane, tag | (u64)f2p(e0_), RLX_AGENT);
    __hip_atomic_store(og + 64 + lane, tag | (u64)f2p(e1_), RLX_AGENT);
    if (lane < 2) __hip_atomic_store(og + 128 + lane, tag | (u64)(lane ? f2p(Ls) : Mp), RLX_AGENT);
  }
  if (!role && wave == 0) {
    const gu64* og = ws + W_OG + (h * 4 + b) * 144;
    u64 g_[8] = {0, 0, 0, 0, 0, 0, 0, 0}, gm = 0, gl = 0;
    while (__hip_atomic_load(cnt + 6, RLX_WG) < 12u) __builtin_amdgcn_s_sleep(16);
    for (unsigned spins = 0; spins < SPIN_CAP; ++spins) {
      gm = __hip_atomic_load(og + 128, RLX_AGENT);
      gl = __hip_atomic_load(og + 129, RLX_AGENT);
      if (__all(tag_ok(gm, tag) && tag_ok(gl, tag))) {
        bool ok = true;
#pragma unroll
        for (int k = 0; k < 8; ++k) {
          g_[k] = __hip_atomic_load(og + c * 8 + k, RLX_AGENT);
          ok &= tag_ok(g_[k], tag);
        }
        if (__all(ok)) break;
      }
      __builtin_amdgcn_s_sleep(16);
    }
    const float mh = p2f(gm), mn = fmaxf(m, mh), ms = (mn == -INFINITY) ? 0.f : mn;
    const float wo_ = __builtin_amdgcn_exp2f(m - ms), wh = r == 0 ? __builtin_amdgcn_exp2f(mh - ms) : 0.f;
    m = mn;
    l = fmaf(wh, p2f(gl), l * wo_);
#pragma unroll
    for (int k = 0; k < 8; ++k) acc[k >> 1][k & 1] = fmaf(wh, p2f(g_[k]), acc[k >> 1][k & 1] * wo_);
  }
  {
    ROWMERGE()
    *(unsigned*)(lds + L_FO + wave * 68 + c * 4 + r) = pack_bf(o0_, o1_);
    if (lane == 0) { lds[L_FM + wave] = m; lds[L_FL + wave] = L_; }
  }

  if (wave >= 8) {
    asm volatile("s_waitcnt lgkmcnt(0)" ::: "memory");
    __builtin_amdgcn_s_barrier();
  } else {
    const int cb = wave & 3, e0 = (wave >> 2) * 4;
    gu64* ydst = ws + W_YG + ((size_t)((cb * 8 + e0 + r) * 4 + b) * 64 + h * 2 + role) * 16 + c;
    while (__hip_atomic_load(cnt + 3, RLX_WG) < 8u) __builtin_amdgcn_s_sleep(4);
    asm volatile("" ::: "memory");
#pragma unroll
    for (int k = 0; k < 8; ++k) {
      A[k] = *(const u32x4*)(lds + L_WF + (((cb * 4 + (k >> 1)) * 8 + e0 + (k & 1)) * 64 + lane) * 4);
      Bf[k] = *(const u32x4*)(lds + L_WF + (((cb * 4 + (k >> 1)) * 8 + e0 + 2 + (k & 1)) * 64 + lane) * 4);
    }
    asm volatile("" : "+v"(ydst));
    asm volatile("s_waitcnt lgkmcnt(0)" ::: "memory");
    __builtin_amdgcn_s_barrier();
    asm volatile("" ::: "memory");
    u32x4 av[4];
#pragma unroll
    for (int kb = 0; kb < 4; ++kb) av[kb] = *(const u32x4*)(lds + L_FO + c * 68 + kb * 16 + r * 4);
    const f32x4 mw = *(const f32x4*)(lds + L_FM + r * 4), lw = *(const f32x4*)(lds + L_FL + r * 4);
    f32x4 d[4];
#pragma unroll
    for (int e = 0; e < 4; ++e) d[e] = f32x4{0.f, 0.f, 0.f, 0.f};
#pragma unroll
    for (int kb = 0; kb < 4; ++kb) {
      d[0] = mfma(av[kb], A[kb * 2], d[0]);
      d[1] = mfma(av[kb], A[kb * 2 + 1], d[1]);
      d[2] = mfma(av[kb], Bf[kb * 2], d[2]);
      d[3] = mfma(av[kb], Bf[kb * 2 + 1], d[3]);
    }
    const float M = xrow_max(fmaxf(fmaxf(mw[0], mw[1]), fmaxf(mw[2], mw[3])));
    float sw[4];
#pragma unroll
    for (int k = 0; k < 4; ++k) sw[k] = (mw[k] == -INFINITY) ? 0.f : __builtin_amdgcn_exp2f(mw[k] - M);
    const float L = xrow_sum((sw[0] * lw[0] + sw[1] * lw[1]) + (sw[2] * lw[2] + sw[3] * lw[3]));
    float ye[4];
#pragma unroll
    for (int e = 0; e < 4; ++e) ye[e] = (sw[0] * d[e][0] + sw[1] * d[e][1]) + (sw[2] * d[e][2] + sw[3] * d[e][3]);
    const float y = tr16_sum(tr32_sum(ye[0], ye[2]), tr32_sum(ye[1], ye[3])) * __builtin_amdgcn_rcpf(L);
    __hip_atomic_store(ydst, tag | (u64)f2p(y), RLX_AGENT);
  }
  __builtin_amdgcn_s_barrier();

  if ((blockIdx.x & 1) == 0) {
    const int rr = blockIdx.x >> 1;
    wait_until(t_org, slot_due(159) + FIN_DELAY);
    u64 g_ = 0;
    for (unsigned spins = 0; spins < SPIN_CAP; ++spins) {
      g_ = __hip_atomic_load(ws + W_YG + (size_t)rr * 1024 + tid, RLX_AGENT);
      if (__all(tag_ok(g_, tag))) break;
      __builtin_amdgcn_s_sleep(1);
    }
    const float v = x32_sum(p2f(g_));
    if (lane < 32) lds[L_QP + lane * 20 + wave] = v;
    __syncthreads();
    if (wave == 0) {
      const float* src = lds + L_QP + (lane & 31) * 20 + (lane >> 5) * 8;
      const f32x4 u = *(const f32x4*)src + *(const f32x4*)(src + 4);
      float t = x32_sum((u[0] + u[1]) + (u[2] + u[3]));
      const float g = lds[L_G + (rr & 3)];
      t = rbf(rbf(t) * ((lane & 16) ? rbf(1.f - g) : g));
      t = x16_sum(t);
      if (lane < 16) a.out[(rr & 3) * DM + (rr >> 5) * 128 + c * 8 + ((rr >> 2) & 7)] = f2bf(t);
      if (blockIdx.x == 0 && lane == 0) __hip_atomic_store(ws + W_EPOCH, epoch + 1ull, RLX_AGENT);
    }
  }
}

}

extern "C" void kernel_launch(void* const* d_in, const int* in_sizes, int n_in, void* d_out, int out_size, void* d_ws,
                              size_t ws_size, hipStream_t stream) {
  (void)in_sizes; (void)n_in; (void)out_size; (void)ws_size;
  Args a;
  a.x = (const char*)d_in[0]; a.gw = (const char*)d_in[10];
  a.kA = (const char*)d_in[1]; a.vA = (const char*)d_in[2]; a.wqA = (const char*)d_in[3]; a.woA = (const char*)d_in[4];
  a.kB = (const char*)d_in[6]; a.vB = (const char*)d_in[7]; a.wqB = (const char*)d_in[8]; a.woB = (const char*)d_in[9];
  a.ws = (u64*)d_ws; a.out = (unsigned short*)d_out;
  hipLaunchKernelGGL(k_fused, dim3(NWG), dim3(NTHR), 0, stream, a);
}
```
